```python
import torch

def _softmax(x, axis=-1):
    return torch.softmax(x, dim=axis)

def _gelu(x):
    return torch.nn.functional.gelu(x, approximate='tanh')

def _silu(x):
    return torch.nn.functional.silu(x)

def _sigmoid(x):
    return torch.sigmoid(x)

def _layernorm(x, axis=-1, eps=1e-05):
    m = x.mean(dim=axis, keepdim=True)
    v = x.var(dim=axis, keepdim=True, unbiased=False)
    return (x - m) / torch.sqrt(v + eps)

def _scatter_add(tgt, idx, src, axis=0):
    return tgt.clone().index_add_(axis, idx, src)

def _segment_sum(src, seg, n):
    out = torch.zeros((n,) + src.shape[1:], dtype=src.dtype)
    return out.index_add_(0, seg, src)

def _pad(x, pw):
    flat = tuple((v for pair in reversed(pw) for v in pair))
    return torch.nn.functional.pad(x, flat)
kernel_name = 'parallel_sum__memory_aug_recurrent_mem_tr__glu_gated_reglu__prompt'

def setup_inputs():

    _w = lambda a: (lambda t: t.to(torch.bfloat16) if t.is_floating_point() else t)(torch.as_tensor(a))

    def _A_setup():
        B, S, Dm = (1, 2048, 640)
        H, Dh = (32, 192)
        M, seg = (8, 32)
        seg = min(seg, S)
        S = S // seg * seg
        return {'x': _w((lambda *s: torch.randn(*s, dtype=torch.bfloat16) * 0.05)(B, S, Dm)), 'mem0': _w((lambda *s: torch.randn(*s, dtype=torch.bfloat16) * 0.2)(B, M, Dm)), 'Wq': _w((lambda *s: torch.randn(*s, dtype=torch.bfloat16) * 1.6)(Dm, H, Dh) * Dm ** (-0.5)), 'Wk': _w((lambda *s: torch.randn(*s, dtype=torch.bfloat16) * 1.6)(Dm, H, Dh) * Dm ** (-0.5)), 'Wv': _w((lambda *s: torch.randn(*s, dtype=torch.bfloat16) * 0.2)(Dm, H, Dh) * Dm ** (-0.5)), 'Wo': _w((lambda *s: torch.randn(*s, dtype=torch.bfloat16) * 0.2)(H, Dh, Dm) * (H * Dh) ** (-0.5)), 'seg_len': seg}
    _A_in = _A_setup()

    def _B_setup():
        B, S, D, F = (1, 2048, 640, 1536)
        return {'x': _w((lambda *s: torch.randn(*s, dtype=torch.bfloat16) * 0.05)(B, S, D)), 'W_gate': _w((lambda *s: torch.randn(*s, dtype=torch.bfloat16) * 0.2)(D, F) * (2.0 / D) ** 0.5), 'W_up': _w((lambda *s: torch.randn(*s, dtype=torch.bfloat16) * 0.2)(D, F) * (2.0 / D) ** 0.5), 'W_down': _w((lambda *s: torch.randn(*s, dtype=torch.bfloat16) * 0.2)(F, D) * (1.0 / F) ** 0.5)}
    _B_in = _B_setup()
    return {'A_x': _A_in['x'], 'A_mem0': _A_in['mem0'], 'A_Wq': _A_in['Wq'], 'A_Wk': _A_in['Wk'], 'A_Wv': _A_in['Wv'], 'A_Wo': _A_in['Wo'], 'A_seg_len': _A_in['seg_len'], 'B_x': _B_in['x'], 'B_W_gate': _B_in['W_gate'], 'B_W_up': _B_in['W_up'], 'B_W_down': _B_in['W_down']}

def reference(A_x, A_mem0, A_Wq, A_Wk, A_Wv, A_Wo, A_seg_len, B_x, B_W_gate, B_W_up, B_W_down):

    def _A_ref(x, mem0, Wq, Wk, Wv, Wo, seg_len):
        B, S, Dm = x.shape
        H, Dh = (Wq.shape[1], Wq.shape[2])
        M = mem0.shape[1]
        scale = Dh ** (-0.5)
        n_seg = S // seg_len
        L = M + seg_len
        idx = torch.arange(L)
        mask = (idx[:, None] < M) | (idx[:, None] >= idx[None, :])
        mem = mem0
        outs = []
        for s in range(n_seg):
            xs = x[:, s * seg_len:(s + 1) * seg_len, :].clone()
            h = torch.concatenate([mem, xs], axis=1)
            q = torch.einsum('btd,dhe->bhte', h, Wq)
            k = torch.einsum('btd,dhe->bhte', h, Wk)
            v = torch.einsum('btd,dhe->bhte', h, Wv)
            scores = torch.einsum('bhqe,bhke->bhqk', q, k) * scale
            scores = torch.where(mask[None, None], scores, -1000000000.0)
            p = _softmax(scores, axis=-1)
            o = torch.einsum('bhqk,bhke->bhqe', p, v)
            o = torch.einsum('bhte,hed->btd', o, Wo)
            mem = o[:, :M, :].clone()
            outs.append(o[:, M:, :])
        return torch.concatenate(outs, axis=1)

    def _B_ref(x, W_gate, W_up, W_down):
        gate = torch.einsum('bsd,df->bsf', x, W_gate)
        up = torch.einsum('bsd,df->bsf', x, W_up)
        gate = gate * (lambda x, dt: x.to(dt))(gate > 0, gate.dtype)
        h = gate * up
        return torch.einsum('bsf,fd->bsd', h, W_down)
    x = A_x
    xn = _layernorm(x, axis=-1)
    return x + _A_ref(xn, A_mem0, A_Wq, A_Wk, A_Wv, A_Wo, A_seg_len) + _B_ref(xn, B_W_gate, B_W_up, B_W_down)
```

```cpp
#include <hip/hip_runtime.h>
#include <stdint.h>

typedef __attribute__((ext_vector_type(2))) __bf16 bf2;
typedef __attribute__((ext_vector_type(4))) __bf16 bf4;
typedef __attribute__((ext_vector_type(8))) __bf16 bf8;
typedef __attribute__((ext_vector_type(16))) __bf16 bf16v;
typedef __attribute__((ext_vector_type(4))) float f4;
typedef __attribute__((ext_vector_type(2))) unsigned u2;
typedef __attribute__((ext_vector_type(4))) unsigned u4;
typedef __attribute__((ext_vector_type(8))) unsigned u8v;
typedef __attribute__((ext_vector_type(4))) short s4v;
typedef __attribute__((address_space(3))) char lchar;
typedef __attribute__((address_space(1))) const void gvoid;
typedef unsigned short u16;

#define DEV __device__ __forceinline__
#define CB() asm volatile("" ::: "memory")
#define SENT 0xFFFFFFFFu

constexpr int NSEG = 64;
constexpr unsigned MB = 1u << 20;
constexpr unsigned OFF_XNP = 0;
constexpr unsigned OFF_D = 4 * MB;
constexpr unsigned OFF_MEM = 32 * MB;
constexpr unsigned OFF_PM = 33 * MB;
constexpr unsigned OFF_FLAG = 35 * MB;
constexpr unsigned OFF_KX = 40 * MB;
constexpr unsigned OFF_QX = 64 * MB;
constexpr unsigned OFF_KM = 88 * MB;
constexpr unsigned OFF_UM = 100 * MB;
constexpr unsigned OFF_PX = 120 * MB;
constexpr unsigned OFF_H = 128 * MB;
constexpr unsigned OFF_B = 136 * MB;
constexpr unsigned OFF_UX = 140 * MB;
constexpr unsigned WS_USED = 220 * MB;
constexpr unsigned FL_KX = 0, FL_KM = 128, FL_PX = 256, FL_H = 512, FL_B = 640, FL_UX = 768, FL_UM = 1152;
constexpr int LOOKAHEAD = 3;
constexpr int SPIN_MAX = 1 << 18;

DEV unsigned pack2(float a, float b) { bf2 v = {(__bf16)a, (__bf16)b}; return __builtin_bit_cast(unsigned, v); }
DEV float bfr(float a) { return (float)(__bf16)a; }
DEV float bflo(unsigned d) { return __builtin_bit_cast(float, d << 16); }
DEV float bfhi(unsigned d) { return __builtin_bit_cast(float, d & 0xFFFF0000u); }
DEV unsigned nosent(unsigned d) { return d < 0xFFFFFFFEu ? d : 0xFFFFFFFEu; }
DEV bf8 as_bf8(u4 v) { return __builtin_bit_cast(bf8, v); }
DEV bf8 lo8(const u8v& v) { u4 t = {v[0], v[1], v[2], v[3]}; return __builtin_bit_cast(bf8, t); }
DEV bf8 hi8(const u8v& v) { u4 t = {v[4], v[5], v[6], v[7]}; return __builtin_bit_cast(bf8, t); }
DEV f4 mfma32(bf8 a, bf8 b, f4 c) { return __builtin_amdgcn_mfma_f32_16x16x32_bf16(a, b, c, 0, 0, 0); }
DEV f4 mfma16(u2 a, u2 b, f4 c) {
  return __builtin_amdgcn_mfma_f32_16x16x16bf16_1k(__builtin_bit_cast(s4v, a), __builtin_bit_cast(s4v, b), c, 0, 0, 0);
}
DEV f4 smf(u4 a, const u8v& b, f4 c, int idx) {
  return __builtin_amdgcn_smfmac_f32_16x16x64_bf16(__builtin_bit_cast(bf8, a), __builtin_bit_cast(bf16v, b), c, idx, 0, 0);
}
DEV u2 trread(lchar* p) {
  bf4 v = __builtin_amdgcn_ds_read_tr16_b64_v4bf16((__attribute__((address_space(3))) bf4*)p);
  return __builtin_bit_cast(u2, v);
}
DEV u4 lds_r128(lchar* p) { return *(__attribute__((address_space(3))) u4*)p; }
DEV void lds_w128(lchar* p, u4 v) { *(__attribute__((address_space(3))) u4*)p = v; }
DEV void lds_w64(lchar* p, u2 v) { *(__attribute__((address_space(3))) u2*)p = v; }
DEV void dma16(const void* g, lchar* l, int sc1) {
  if (sc1) __builtin_amdgcn_global_load_lds((gvoid*)g, (__attribute__((address_space(3))) void*)l, 16, 0, 16);
  else __builtin_amdgcn_global_load_lds((gvoid*)g, (__attribute__((address_space(3))) void*)l, 16, 0, 0);
}
typedef __amdgpu_buffer_rsrc_t rsrc_t;
DEV u4 ld128(rsrc_t r, unsigned v, unsigned s) { CB(); u4 x = __builtin_amdgcn_raw_buffer_load_b128(r, v, s, 16); CB(); return x; }
DEV u2 ld64(rsrc_t r, unsigned v, unsigned s) { CB(); u2 x = __builtin_amdgcn_raw_buffer_load_b64(r, v, s, 16); CB(); return x; }
DEV unsigned ld32(rsrc_t r, unsigned v, unsigned s) { CB(); unsigned x = __builtin_amdgcn_raw_buffer_load_b32(r, v, s, 16); CB(); return x; }
DEV void st128(u4 d, rsrc_t r, unsigned v, unsigned s) { __builtin_amdgcn_raw_buffer_store_b128(d, r, v, s, 16); }
DEV void st64(u2 d, rsrc_t r, unsigned v, unsigned s) { __builtin_amdgcn_raw_buffer_store_b64(d, r, v, s, 16); }
DEV void st32(unsigned d, rsrc_t r, unsigned v, unsigned s) { __builtin_amdgcn_raw_buffer_store_b32(d, r, v, s, 16); }
DEV void wait_vm0() { asm volatile("s_waitcnt vmcnt(0)" ::: "memory"); }
DEV void wait_lgkm0() { asm volatile("s_waitcnt lgkmcnt(0)" ::: "memory"); }
DEV void bar() { asm volatile("s_waitcnt lgkmcnt(0)\n\ts_barrier" ::: "memory"); }
DEV unsigned umax4(u4 v) { return max(max(v[0], v[1]), max(v[2], v[3])); }
DEV float u2f(unsigned u) { return __builtin_bit_cast(float, u); }
DEV unsigned f2u(float f) { return __builtin_bit_cast(unsigned, f); }
DEV float xmax32(float v) { u2 r = __builtin_amdgcn_permlane32_swap(f2u(v), f2u(v), false, false); unsigned a = r[0], b = r[1]; return fmaxf(u2f(a), u2f(b)); }
DEV float xmax16(float v) { u2 r = __builtin_amdgcn_permlane16_swap(f2u(v), f2u(v), false, false); unsigned a = r[0], b = r[1]; return fmaxf(u2f(a), u2f(b)); }
DEV float xsum32(float v) { u2 r = __builtin_amdgcn_permlane32_swap(f2u(v), f2u(v), false, false); unsigned a = r[0], b = r[1]; return u2f(a) + u2f(b); }
DEV float xsum16(float v) { u2 r = __builtin_amdgcn_permlane16_swap(f2u(v), f2u(v), false, false); unsigned a = r[0], b = r[1]; return u2f(a) + u2f(b); }
DEV float halfsum(float a, float b) {
  u2 r = __builtin_amdgcn_permlane32_swap(f2u(a), f2u(b), false, false);
  unsigned x = r[0], y = r[1];
  return u2f(x) + u2f(y);
}

DEV void wait_flag(rsrc_t r, unsigned voff, unsigned soff, bool& dead) {
  if (dead) return;
  for (int i = 0; i < SPIN_MAX; ++i) {
    unsigned f = ld32(r, voff, soff);
    if (__all(f != 0)) return;
    __builtin_amdgcn_s_sleep(1);
  }
  dead = true;
}

__global__ void __launch_bounds__(256) k_prep(const u16* __restrict__ x, const u16* __restrict__ mem0, char* __restrict__ ws) {
  const int tid = threadIdx.x, l = tid & 63, w = tid >> 6;
  const int b = blockIdx.x;
  if (b < 512) {
    const int row = b * 4 + w;
    u4 d0 = {0, 0, 0, 0}, d1 = {0, 0, 0, 0};
    const bool act = l < 40;
    if (act) {
      const u4* p = (const u4*)(x + (size_t)row * 640 + l * 16);
      d0 = p[0]; d1 = p[1];
    }
    float v[16];
#pragma unroll
    for (int i = 0; i < 4; i++) { v[2 * i] = bflo(d0[i]); v[2 * i + 1] = bfhi(d0[i]); v[8 + 2 * i] = bflo(d1[i]); v[8 + 2 * i + 1] = bfhi(d1[i]); }
    float s = 0;
#pragma unroll
    for (int i = 0; i < 16; i++) s += v[i];
#pragma unroll
    for (int o = 32; o >= 1; o >>= 1) s += __shfl_xor(s, o);
    const float mean = s * (1.0f / 640.0f);
    float q = 0;
    if (act) {
#pragma unroll
      for (int i = 0; i < 16; i++) { float d = v[i] - mean; q += d * d; }
    }
#pragma unroll
    for (int o = 32; o >= 1; o >>= 1) q += __shfl_xor(q, o);
    const float mb = bfr(mean);
    const float vb = bfr(q * (1.0f / 640.0f));
    const float ve = bfr(vb + 1e-5f);
    const float sd = bfr(sqrtf(ve));
    if (act) {
      unsigned o[8];
#pragma unroll
      for (int i = 0; i < 8; i++) o[i] = pack2(bfr(v[2 * i] - mb) / sd, bfr(v[2 * i + 1] - mb) / sd);
      const int seg = row >> 5, rr = row & 31;
      const int n = 4 * (rr >> 3) + (rr & 3), rt = (rr >> 2) & 1;
      const int kb32 = l >> 1, g0 = 2 * (l & 1);
      char* dst = ws + OFF_XNP + (size_t)(((seg * 2 + rt) * 20 + kb32) * 1024) + (g0 * 16 + n) * 16;
      u4 c0 = {o[0], o[4], o[1], o[5]}, c1 = {o[2], o[6], o[3], o[7]};
      *(u4*)dst = c0;
      *(u4*)(dst + 256) = c1;
    }
  } else {
    const int fb = b - 512;
    const unsigned gt = fb * 256 + tid;
    const u4 sv = {SENT, SENT, SENT, SENT}, zv = {0, 0, 0, 0};
    for (unsigned i = gt; i < (63 * 10240) / 16; i += 32768) *(u4*)(ws + OFF_MEM + 10240 + (size_t)i * 16) = sv;
    for (unsigned i = gt; i < (64 * 20480) / 16; i += 32768) *(u4*)(ws + OFF_PM + (size_t)i * 16) = sv;
    for (unsigned i = gt; i < (64 * 2048) / 16; i += 32768) *(u4*)(ws + OFF_FLAG + (size_t)i * 16) = zv;
    if (gt < 640) {
      const int j = gt >> 3, r = gt & 7;
      u4 d = *(const u4*)(mem0 + r * 640 + j * 8);
      d[0] = nosent(d[0]); d[1] = nosent(d[1]); d[2] = nosent(d[2]); d[3] = nosent(d[3]);
      *(u4*)(ws + OFF_MEM + gt * 16) = d;
    }
  }
}

__global__ void __launch_bounds__(256) k_dgemm(const u16* __restrict__ Wv, const u16* __restrict__ Wo, char* __restrict__ ws) {
  __shared__ __attribute__((aligned(16))) char smem[192 * 256];
  lchar* L = (lchar*)smem;
  const int tid = threadIdx.x, l = tid & 63, w = __builtin_amdgcn_readfirstlane(tid >> 6);
  const int b = blockIdx.x;
  const int h = b / 25, mt = (b % 25) / 5, nt5 = b % 5;
#pragma unroll
  for (int i = 0; i < 12; i++) {
    const int slot = i * 256 + tid, r = slot >> 4, c16 = slot & 15;
    u4 d = *(const u4*)(Wo + ((size_t)h * 192 + r) * 640 + nt5 * 128 + c16 * 8);
    const int f = (r & 3) | ((r >> 1) & 4);
    lds_w128(L + r * 256 + (((c16 >> 1) ^ f) * 32) + (c16 & 1) * 16, d);
  }
  const int m = l & 15, g = l >> 4;
  const int kb = mt * 2 + (w >> 1), half = w & 1;
  const int mg = m >> 2, mi = m & 3;
  const int din = 64 * kb + 32 * half + 16 * (mg >> 1) + 4 * (mg & 1) + 8 * (mi >> 1) + (mi & 1);
  const u16* pa = Wv + (size_t)din * 6144 + h * 192 + g * 8;
  u4 A[6][2];
#pragma unroll
  for (int u = 0; u < 6; u++) { A[u][0] = *(const u4*)(pa + u * 32); A[u][1] = *(const u4*)(pa + 2 * 6144 + u * 32); }
  __syncthreads();
  f4 acc[2][8];
#pragma unroll
  for (int i = 0; i < 8; i++) { acc[0][i] = (f4){0, 0, 0, 0}; acc[1][i] = (f4){0, 0, 0, 0}; }
  const int q = (l >> 2) & 3, p = l & 3;
#pragma unroll
  for (int u = 0; u < 6; u++) {
#pragma unroll
    for (int nt = 0; nt < 8; nt++) {
      const int r0 = 32 * u + 8 * g + q, r1 = r0 + 4;
      const int f = q | ((r0 >> 1) & 4);
      u2 b0 = trread(L + r0 * 256 + ((nt ^ f) * 32) + p * 8);
      u2 b1 = trread(L + r1 * 256 + ((nt ^ f) * 32) + p * 8);
      u4 B = {b0[0], b0[1], b1[0], b1[1]};
      acc[0][nt] = mfma32(as_bf8(A[u][0]), as_bf8(B), acc[0][nt]);
      acc[1][nt] = mfma32(as_bf8(A[u][1]), as_bf8(B), acc[1][nt]);
    }
  }
  const int hsel = h >> 4, hh = h & 15;
#pragma unroll
  for (int nt = 0; nt < 8; nt++) {
    const int col = nt5 * 128 + nt * 16 + m;
    const int j = col >> 3, c = col & 7;
    u4 o = {pack2(acc[0][nt][0], acc[0][nt][1]), pack2(acc[0][nt][2], acc[0][nt][3]), pack2(acc[1][nt][0], acc[1][nt][1]), pack2(acc[1][nt][2], acc[1][nt][3])};
    size_t off = (size_t)j * 327680 + (size_t)(((hh * 10 + kb) * 2 + half) * 1024) + (hsel * 8 + c + 16 * g) * 16;
    *(u4*)(ws + OFF_D + off) = o;
  }
}

struct Ctx { lchar* L; char* ws; rsrc_t rs; int tid, l, w; };

DEV int swap13(int r) { return (r & ~10) | ((r >> 2) & 2) | ((r << 2) & 8); }

DEV void gather_mem(const Ctx& c, int s, lchar* MX, bool& dead, u4 (&A)[10]) {
  const unsigned so = OFF_MEM + s * 10240;
  u4 a0, a1; u2 a2;
  int it = 0;
  while (true) {
    a0 = ld128(c.rs, c.tid * 16, so);
    a1 = ld128(c.rs, 4096 + c.tid * 16, so);
    a2 = ld64(c.rs, 8192 + c.tid * 8, so);
    unsigned mx = max(max(umax4(a0), umax4(a1)), max(a2[0], a2[1]));
    if (__all(mx != SENT) || dead) break;
    if (++it > SPIN_MAX) { dead = true; break; }
  }
  lds_w128(MX + c.tid * 16, a0);
  lds_w128(MX + 4096 + c.tid * 16, a1);
  lds_w64(MX + 8192 + c.tid * 8, a2);
  bar();
#pragma unroll
  for (int i = 0; i < 10; i++) A[i] = lds_r128(MX + i * 1024 + c.l * 16);
}

constexpr int IMG_QK = 53248;
DEV void qk_dma(const Ctx& c, const u16* Wq, const u16* Wk, int h, int kb, lchar* buf) {
#pragma unroll
  for (int i = 0; i < 13; i++) {
    const int slot = i * 256 + c.tid;
    const int row = slot / 26, cc = slot - row * 26;
    const int k = 64 * kb + swap13(row & 63);
    const u16* src = ((row >> 6) ? Wk : Wq) + (size_t)k * 6144 + h * 192 + (cc < 24 ? cc : 23) * 8;
    dma16(src, buf + (i * 256 + c.w * 64) * 16, 0);
  }
}
DEV u8v qk_frag(lchar* lb, int tt) {
  const int o = (tt / 3) * 64 * 416 + (tt % 3) * 32;
  u2 a = trread(lb + o), b = trread(lb + o + 8 * 416), cc = trread(lb + o + 32 * 416), d = trread(lb + o + 40 * 416);
  u8v r = {a[0], a[1], b[0], b[1], cc[0], cc[1], d[0], d[1]};
  return r;
}
DEV int qk_lanebase(int l, int w) {
  const int gb = l >> 4, q = (l >> 2) & 3, p = l & 3;
  return (16 * (gb >> 1) + 4 * (gb & 1) + q) * 416 + (48 * w + 4 * p) * 2;
}
DEV void qk_load(const Ctx& c, const u16* Wq, const u16* Wk, int h, u8v (&W)[6][8]) {
  const int lb = qk_lanebase(c.l, c.w);
  qk_dma(c, Wq, Wk, h, 0, c.L);
  qk_dma(c, Wq, Wk, h, 1, c.L + IMG_QK);
  qk_dma(c, Wq, Wk, h, 2, c.L + 2 * IMG_QK);
#pragma unroll
  for (int kb = 0; kb < 8; kb++) {
    if (kb + 2 < 10) asm volatile("s_waitcnt vmcnt(26)" ::: "memory"); else wait_vm0();
    bar();
#pragma unroll
    for (int tt = 0; tt < 6; tt++) W[tt][kb] = qk_frag(c.L + (kb % 3) * IMG_QK + lb, tt);
    bar();
    if (kb + 3 < 10) qk_dma(c, Wq, Wk, h, kb + 3, c.L + (kb % 3) * IMG_QK);
  }
  wait_vm0();
  bar();
}

constexpr float SCALE = 0.07216878364870322f;

DEV void role_S(const Ctx& c, int h, const u16* Wq, const u16* Wk) {
  bool dead = false;
  u8v W[6][8];
  qk_load(c, Wq, Wk, h, W);
  lchar* KXB = c.L + IMG_QK;
  lchar* QKI = KXB + 24576;
  lchar* MX = QKI + 6144;
  lchar* w8 = c.L + 2 * IMG_QK + qk_lanebase(c.l, c.w);
  lchar* w9 = c.L + qk_lanebase(c.l, c.w);
  const int l = c.l, g = l >> 4, n = l & 15;
  const int idx = (l & 8) ? 0xEEEE : 0x4444;
  const char* kxsrc = c.ws + OFF_KX + (size_t)h * 12288 + c.l * 16;
  if (c.w > 0) {
    wait_flag(c.rs, FL_KX + h * 4, OFF_FLAG, dead);
#pragma unroll
    for (int i = 0; i < 4; i++) { const int fr = (c.w - 1) * 4 + i; dma16(kxsrc + fr * 1024, KXB + fr * 1024, 1); }
  }
  for (int s = 0; s < NSEG; s++) {
    u4 A[10];
    gather_mem(c, s, MX, dead, A);
    f4 acc[6];
#pragma unroll
    for (int tt = 0; tt < 6; tt++) acc[tt] = (f4){0, 0, 0, 0};
#pragma unroll
    for (int kb = 0; kb < 8; kb++)
#pragma unroll
      for (int tt = 0; tt < 6; tt++) acc[tt] = smf(A[kb], W[tt][kb], acc[tt], idx);
#pragma unroll
    for (int tt = 0; tt < 6; tt++) { u8v f = qk_frag(w8, tt); acc[tt] = smf(A[8], f, acc[tt], idx); }
#pragma unroll
    for (int tt = 0; tt < 6; tt++) { u8v f = qk_frag(w9, tt); acc[tt] = smf(A[9], f, acc[tt], idx); }
#pragma unroll
    for (int t = 0; t < 3; t++) {
      float v0 = halfsum(acc[t][0], acc[t + 3][0]), v1 = halfsum(acc[t][1], acc[t + 3][1]);
      float v2 = halfsum(acc[t][2], acc[t + 3][2]), v3 = halfsum(acc[t][3], acc[t + 3][3]);
      u2 o = {pack2(v0, v1), pack2(v2, v3)};
      lds_w64(QKI + (48 * c.w + 16 * t + n) * 32 + g * 8, o);
    }
    bar();
    if (c.w == 0) {
      lchar* kx = KXB + (s & 1) * 12288 + l * 16;
      u4 F[6];
      lchar* qb = QKI + (4 * g + ((l >> 2) & 3)) * 32 + (l & 3) * 8;
#pragma unroll
      for (int u = 0; u < 6; u++) { u2 a = trread(qb + u * 1024), b = trread(qb + u * 1024 + 512); F[u] = (u4){a[0], a[1], b[0], b[1]}; }
      if (s < NSEG - 1) {
        f4 T0 = {0, 0, 0, 0}, T1 = T0, T2 = T0;
#pragma unroll
        for (int u = 0; u < 6; u++) {
          T0 = mfma32(as_bf8(F[u]), as_bf8(F[u]), T0);
          T1 = mfma32(as_bf8(lds_r128(kx + u * 1024)), as_bf8(F[u]), T1);
          T2 = mfma32(as_bf8(lds_r128(kx + 6144 + u * 1024)), as_bf8(F[u]), T2);
        }
        float v[12];
#pragma unroll
        for (int i = 0; i < 4; i++) {
          v[i] = g >= 2 ? bfr(bfr(T0[i]) * SCALE) : -3.0e38f;
          v[4 + i] = bfr(bfr(T1[i]) * SCALE);
          v[8 + i] = bfr(bfr(T2[i]) * SCALE);
        }
        float mx = v[0];
#pragma unroll
        for (int i = 1; i < 12; i++) mx = fmaxf(mx, v[i]);
        mx = xmax32(xmax16(mx));
        float sum = 0;
#pragma unroll
        for (int i = 0; i < 12; i++) { v[i] = __expf(v[i] - mx); sum += v[i]; }
        sum = xsum32(xsum16(sum));
        const float inv = 1.0f / sum;
        u4 px = {nosent(pack2(v[4] * inv, v[5] * inv)), nosent(pack2(v[6] * inv, v[7] * inv)), nosent(pack2(v[8] * inv, v[9] * inv)), nosent(pack2(v[10] * inv, v[11] * inv))};
        u2 pm = {nosent(pack2(v[0] * inv, v[1] * inv)), nosent(pack2(v[2] * inv, v[3] * inv))};
        const unsigned so = OFF_PM + s * 20480 + h * 640;
        if (n < 8) {
          st128(px, c.rs, g * 128 + n * 16, so);
          if (g >= 2) st64(pm, c.rs, 512 + (g - 2) * 64 + n * 8, so);
        }
      }
      const unsigned ko = OFF_KM + (s * 32 + h) * 6144;
#pragma unroll
      for (int u = 0; u < 6; u++) st128(F[u], c.rs, u * 1024 + l * 16, ko);
      wait_vm0();
      if (l == 0) st32(1u, c.rs, FL_KM + h * 4, OFF_FLAG + s * 2048);
    } else if (s + 1 < NSEG - 1) {
      wait_flag(c.rs, FL_KX + h * 4, OFF_FLAG + (s + 1) * 2048, dead);
      const char* src = kxsrc + (size_t)(s + 1) * (32 * 12288);
#pragma unroll
      for (int i = 0; i < 4; i++) { const int fr = (c.w - 1) * 4 + i; dma16(src + fr * 1024, KXB + ((s + 1) & 1) * 12288 + fr * 1024, 1); }
    }
  }
}

DEV void d_load(const Ctx& c, int j, u8v (&D)[4][10]) {
  const char* src = c.ws + OFF_D + (size_t)j * 327680 + (size_t)c.w * 81920 + c.l * 16;
#pragma unroll
  for (int t = 0; t < 4; t++)
#pragma unroll
    for (int kb = 0; kb < 10; kb++) {
      u4 a = *(const u4*)(src + (t * 10 + kb) * 2048), b = *(const u4*)(src + (t * 10 + kb) * 2048 + 1024);
      D[t][kb] = (u8v){a[0], a[1], a[2], a[3], b[0], b[1], b[2], b[3]};
    }
}

DEV void role_R(const Ctx& c, int j) {
  bool dead = false;
  u8v D[4][10];
  d_load(c, j, D);
  lchar* MX = c.L;
  lchar* RED = c.L + 10240;
  const int l = c.l, g = l >> 4, n = l & 15, w = c.w;
  const int idx = (l & 8) ? 0xEEEE : 0x4444;
  const unsigned pxo = (4 * w + 16 * (n >> 3)) * 640 + g * 128 + (n & 7) * 16;
  const unsigned pmo = (4 * w + (g >> 1) + 16 * (n >> 3)) * 640 + 512 + (g & 1) * 64 + (n & 7) * 8;
  const unsigned uxo = j * 16384 + w * 4096 + l * 16;
  u4 UX[4];
  wait_flag(c.rs, FL_UX + j * 4, OFF_FLAG, dead);
#pragma unroll
  for (int t = 0; t < 4; t++) UX[t] = ld128(c.rs, uxo + t * 1024, OFF_UX);
  for (int s = 0; s < NSEG; s++) {
    u4 A[10];
    gather_mem(c, s, MX, dead, A);
    f4 acc[4];
#pragma unroll
    for (int t = 0; t < 4; t++) acc[t] = (f4){0, 0, 0, 0};
#pragma unroll
    for (int kb = 0; kb < 10; kb++)
#pragma unroll
      for (int t = 0; t < 4; t++) acc[t] = smf(A[kb], D[t][kb], acc[t], idx);
    u2 ZM[2];
#pragma unroll
    for (int pp = 0; pp < 2; pp++) {
      float v0 = halfsum(acc[2 * pp][0], acc[2 * pp + 1][0]), v1 = halfsum(acc[2 * pp][1], acc[2 * pp + 1][1]);
      float v2 = halfsum(acc[2 * pp][2], acc[2 * pp + 1][2]), v3 = halfsum(acc[2 * pp][3], acc[2 * pp + 1][3]);
      ZM[pp] = (u2){pack2(v0, v1), pack2(v2, v3)};
      st64(ZM[pp], c.rs, (w * 2 + pp) * 512 + l * 8, OFF_UM + (s * 80 + j) * 4096);
    }
    if (s < NSEG - 1) {
      const unsigned so = OFF_PM + s * 20480;
      u4 P[4]; u2 PM[2];
      int it = 0;
      while (true) {
#pragma unroll
        for (int t = 0; t < 4; t++) P[t] = ld128(c.rs, pxo + t * 640, so);
#pragma unroll
        for (int pp = 0; pp < 2; pp++) PM[pp] = ld64(c.rs, pmo + pp * 1280, so);
        unsigned mx = max(max(umax4(P[0]), umax4(P[1])), max(umax4(P[2]), umax4(P[3])));
        mx = max(mx, max(max(PM[0][0], PM[0][1]), max(PM[1][0], PM[1][1])));
        if (__all(mx != SENT) || dead) break;
        if (++it > SPIN_MAX) { dead = true; break; }
      }
      f4 o0 = {0, 0, 0, 0}, o1 = o0;
      o0 = mfma32(as_bf8(UX[0]), as_bf8(P[0]), o0);
      o1 = mfma32(as_bf8(UX[1]), as_bf8(P[1]), o1);
      o0 = mfma32(as_bf8(UX[2]), as_bf8(P[2]), o0);
      o1 = mfma32(as_bf8(UX[3]), as_bf8(P[3]), o1);
      o0 = mfma16(ZM[0], PM[0], o0);
      o1 = mfma16(ZM[1], PM[1], o1);
      o0 += o1;
      if ((n >> 3) == (g >> 1)) lds_w128(RED + w * 512 + (g >> 1) * 256 + (n & 7) * 32 + (g & 1) * 16, __builtin_bit_cast(u4, o0));
      bar();
      if (w == 0) {
        if (l < 16) {
          f4 t = {0, 0, 0, 0};
#pragma unroll
          for (int i = 0; i < 8; i++) t += __builtin_bit_cast(f4, lds_r128(RED + i * 256 + l * 16));
          u2 o = {nosent(pack2(t[0], t[1])), nosent(pack2(t[2], t[3]))};
          st64(o, c.rs, j * 128 + l * 8, OFF_MEM + (s + 1) * 10240);
        }
      } else if (w == 1) {
        if (l == 0) st32(1u, c.rs, FL_UM + j * 4, OFF_FLAG + s * 2048);
      }
      if (s + 1 < NSEG - 1) {
        wait_flag(c.rs, FL_UX + j * 4, OFF_FLAG + (s + 1) * 2048, dead);
#pragma unroll
        for (int t = 0; t < 4; t++) UX[t] = ld128(c.rs, uxo + t * 1024, OFF_UX + (s + 1) * (80 * 16384));
      }
    } else {
      wait_vm0();
      bar();
      if (c.tid == 0) st32(1u, c.rs, FL_UM + j * 4, OFF_FLAG + s * 2048);
    }
  }
}

DEV void role_X(const Ctx& c, int h, const u16* Wq, const u16* Wk) {
  bool dead = false;
  u8v W[6][8];
  qk_load(c, Wq, Wk, h, W);
  lchar* XB = c.L + IMG_QK;
  lchar* w8 = c.L + 2 * IMG_QK + qk_lanebase(c.l, c.w);
  lchar* w9 = c.L + qk_lanebase(c.l, c.w);
  const int l = c.l, g = l >> 4, n = l & 15, w = c.w;
  const char* xsrc = c.ws + OFF_XNP + c.tid * 16;
#pragma unroll
  for (int i = 0; i < 5; i++) dma16(xsrc + i * 4096, XB + i * 4096 + w * 1024, 0);
  for (int it = 0; it < NSEG + LOOKAHEAD; it++) {
    if (it < NSEG) {
      const int s = it;
#pragma unroll
      for (int rt = 0; rt < 2; rt++) {
        wait_vm0();
        bar();
        if (rt == 0 && s > 0 && c.tid == 0) st32(1u, c.rs, FL_KX + h * 4, OFF_FLAG + (s - 1) * 2048);
        const int nx = s * 2 + rt + 1;
        if (nx < 2 * NSEG) {
#pragma unroll
          for (int i = 0; i < 5; i++) dma16(xsrc + (size_t)nx * 20480 + i * 4096, XB + (rt ^ 1) * 20480 + i * 4096 + w * 1024, 0);
        }
        lchar* xb = XB + rt * 20480 + l * 16;
        f4 acc[6];
#pragma unroll
        for (int tt = 0; tt < 6; tt++) acc[tt] = (f4){0, 0, 0, 0};
#pragma unroll
        for (int kb = 0; kb < 8; kb++) {
          bf8 b0 = as_bf8(lds_r128(xb + kb * 2048)), b1 = as_bf8(lds_r128(xb + kb * 2048 + 1024));
#pragma unroll
          for (int tt = 0; tt < 6; tt++) acc[tt] = mfma32(lo8(W[tt][kb]), b0, acc[tt]);
#pragma unroll
          for (int tt = 0; tt < 6; tt++) acc[tt] = mfma32(hi8(W[tt][kb]), b1, acc[tt]);
        }
#pragma unroll
        for (int kb = 8; kb < 10; kb++) {
          bf8 b0 = as_bf8(lds_r128(xb + kb * 2048)), b1 = as_bf8(lds_r128(xb + kb * 2048 + 1024));
#pragma unroll
          for (int tt = 0; tt < 6; tt++) {
            u8v f = qk_frag(kb == 8 ? w8 : w9, tt);
            acc[tt] = mfma32(lo8(f), b0, acc[tt]);
            acc[tt] = mfma32(hi8(f), b1, acc[tt]);
          }
        }
#pragma unroll
        for (int tt = 0; tt < 6; tt++) {
          const int t = 3 * w + tt % 3;
          u2 o = {pack2(acc[tt][0], acc[tt][1]), pack2(acc[tt][2], acc[tt][3])};
          st64(o, c.rs, rt * 6144 + (t >> 1) * 1024 + l * 16 + (t & 1) * 8, (tt < 3 ? OFF_QX : OFF_KX) + (s * 32 + h) * 12288);
        }
      }
      if (s == NSEG - 1) {
        wait_vm0();
        bar();
      }
    }
    if (it >= LOOKAHEAD && w < 2) {
      const int s = it - LOOKAHEAD, rt = w;
      wait_flag(c.rs, FL_KM + h * 4, OFF_FLAG + s * 2048, dead);
      const unsigned qo = OFF_QX + (s * 32 + h) * 12288 + rt * 6144, ko = OFF_KX + (s * 32 + h) * 12288, mo = OFF_KM + (s * 32 + h) * 6144;
      f4 T0 = {0, 0, 0, 0}, T1 = T0, T2 = T0;
#pragma unroll
      for (int uu = 0; uu < 2; uu++) {
        u4 Q[3], K0[3], K1[3], K2[3];
#pragma unroll
        for (int i = 0; i < 3; i++) {
          const unsigned vo = (uu * 3 + i) * 1024 + l * 16;
          Q[i] = ld128(c.rs, vo, qo); K0[i] = ld128(c.rs, vo, mo); K1[i] = ld128(c.rs, vo, ko); K2[i] = ld128(c.rs, vo + 6144, ko);
        }
#pragma unroll
        for (int i = 0; i < 3; i++) {
          T0 = mfma32(as_bf8(K0[i]), as_bf8(Q[i]), T0);
          T1 = mfma32(as_bf8(K1[i]), as_bf8(Q[i]), T1);
          T2 = mfma32(as_bf8(K2[i]), as_bf8(Q[i]), T2);
        }
      }
      const int qi = 8 * (n >> 2) + 4 * rt + (n & 3);
      float v[12];
#pragma unroll
      for (int i = 0; i < 4; i++) {
        v[i] = g >= 2 ? bfr(bfr(T0[i]) * SCALE) : -3.0e38f;
        v[4 + i] = (8 * g + i <= qi) ? bfr(bfr(T1[i]) * SCALE) : -3.0e38f;
        v[8 + i] = (8 * g + 4 + i <= qi) ? bfr(bfr(T2[i]) * SCALE) : -3.0e38f;
      }
      float mx = v[0];
#pragma unroll
      for (int i = 1; i < 12; i++) mx = fmaxf(mx, v[i]);
      mx = xmax32(xmax16(mx));
      float sum = 0;
#pragma unroll
      for (int i = 0; i < 12; i++) { v[i] = __expf(v[i] - mx); sum += v[i]; }
      sum = xsum32(xsum16(sum));
      const float inv = 1.0f / sum;
      u4 px = {pack2(v[4] * inv, v[5] * inv), pack2(v[6] * inv, v[7] * inv), pack2(v[8] * inv, v[9] * inv), pack2(v[10] * inv, v[11] * inv)};
      u2 pm = {pack2(v[0] * inv, v[1] * inv), pack2(v[2] * inv, v[3] * inv)};
      const unsigned so = OFF_PX + (s * 32 + h) * 2560 + rt * 1280;
      st128(px, c.rs, g * 256 + n * 16, so);
      if (g >= 2) st64(pm, c.rs, 1024 + (g - 2) * 128 + n * 8, so);
      wait_vm0();
      if (l == 0) st32(1u, c.rs, FL_PX + (h * 2 + rt) * 4, OFF_FLAG + s * 2048);
    }
  }
}

DEV void role_R2(const Ctx& c, int j, const u16* x, u16* out) {
  bool dead = false;
  u8v D[4][10];
  d_load(c, j, D);
  lchar* XB = c.L;
  lchar* RED = c.L + 81920;
  const int l = c.l, g = l >> 4, n = l & 15, w = c.w;
  const char* xsrc = c.ws + OFF_XNP + c.tid * 16;
#pragma unroll
  for (int i = 0; i < 10; i++) dma16(xsrc + i * 4096, XB + i * 4096 + w * 1024, 0);
  const unsigned uxo = j * 16384 + w * 4096 + l * 16;
  const unsigned pxo = (4 * w + 16 * (n >> 3)) * 2560 + g * 256 + (n & 7) * 16;
  const unsigned pmo = (4 * w + (g >> 1) + 16 * (n >> 3)) * 2560 + 1024 + (g & 1) * 128 + (n & 7) * 8;
  for (int it = 0; it < NSEG + LOOKAHEAD; it++) {
    if (it < NSEG) {
      const int s = it;
      wait_vm0();
      bar();
      if (s > 0 && c.tid == 0) st32(1u, c.rs, FL_UX + j * 4, OFF_FLAG + (s - 1) * 2048);
      if (s + 1 < NSEG) {
#pragma unroll
        for (int i = 0; i < 10; i++) dma16(xsrc + (size_t)(s + 1) * 40960 + i * 4096, XB + ((s + 1) & 1) * 40960 + i * 4096 + w * 1024, 0);
      }
      lchar* xb = XB + (s & 1) * 40960 + l * 16;
      f4 acc[2][4];
#pragma unroll
      for (int t = 0; t < 4; t++) { acc[0][t] = (f4){0, 0, 0, 0}; acc[1][t] = (f4){0, 0, 0, 0}; }
#pragma unroll
      for (int kb = 0; kb < 10; kb++) {
        bf8 a00 = as_bf8(lds_r128(xb + kb * 2048)), a01 = as_bf8(lds_r128(xb + kb * 2048 + 1024));
        bf8 a10 = as_bf8(lds_r128(xb + 20480 + kb * 2048)), a11 = as_bf8(lds_r128(xb + 20480 + kb * 2048 + 1024));
#pragma unroll
        for (int t = 0; t < 4; t++) { acc[0][t] = mfma32(a00, lo8(D[t][kb]), acc[0][t]); acc[1][t] = mfma32(a10, lo8(D[t][kb]), acc[1][t]); }
#pragma unroll
        for (int t = 0; t < 4; t++) { acc[0][t] = mfma32(a01, hi8(D[t][kb]), acc[0][t]); acc[1][t] = mfma32(a11, hi8(D[t][kb]), acc[1][t]); }
      }
#pragma unroll
      for (int t = 0; t < 4; t++) {
        u4 o = {pack2(acc[0][t][0], acc[0][t][1]), pack2(acc[0][t][2], acc[0][t][3]), pack2(acc[1][t][0], acc[1][t][1]), pack2(acc[1][t][2], acc[1][t][3])};
        st128(o, c.rs, uxo + t * 1024, OFF_UX + s * (80 * 16384));
      }
      if (s == NSEG - 1) wait_vm0();
    }
    if (it >= LOOKAHEAD) {
      const int s = it - LOOKAHEAD;
      const unsigned fo = OFF_FLAG + s * 2048;
      wait_flag(c.rs, FL_UM + j * 4, fo, dead);
      wait_flag(c.rs, FL_PX + l * 4, fo, dead);
      u4 UX[4]; u2 ZM[2];
#pragma unroll
      for (int t = 0; t < 4; t++) UX[t] = ld128(c.rs, uxo + t * 1024, OFF_UX + s * (80 * 16384));
#pragma unroll
      for (int pp = 0; pp < 2; pp++) ZM[pp] = ld64(c.rs, (w * 2 + pp) * 512 + l * 8, OFF_UM + (s * 80 + j) * 4096);
      const unsigned so = OFF_PX + s * (32 * 2560);
#pragma unroll
      for (int rg = 0; rg < 4; rg++) {
        const int rt = rg >> 1, half = rg & 1;
        u4 P[4]; u2 PM[2];
#pragma unroll
        for (int t = 0; t < 4; t++) P[t] = ld128(c.rs, pxo + t * 2560 + rt * 1280 + half * 128, so);
#pragma unroll
        for (int pp = 0; pp < 2; pp++) PM[pp] = ld64(c.rs, pmo + pp * 5120 + rt * 1280 + half * 64, so);
        f4 o0 = {0, 0, 0, 0}, o1 = o0;
        o0 = mfma32(as_bf8(UX[0]), as_bf8(P[0]), o0);
        o1 = mfma32(as_bf8(UX[1]), as_bf8(P[1]), o1);
        o0 = mfma32(as_bf8(UX[2]), as_bf8(P[2]), o0);
        o1 = mfma32(as_bf8(UX[3]), as_bf8(P[3]), o1);
        o0 = mfma16(ZM[0], PM[0], o0);
        o1 = mfma16(ZM[1], PM[1], o1);
        o0 += o1;
        if ((n >> 3) == (g >> 1)) lds_w128(RED + rg * 2048 + w * 512 + (g >> 1) * 256 + (n & 7) * 32 + (g & 1) * 16, __builtin_bit_cast(u4, o0));
      }
      if (w == 0) wait_flag(c.rs, FL_B + (j >> 3) * 4, fo, dead);
      bar();
      if (w == 0) {
        const int rg = l >> 4, r8 = (l >> 1) & 7, ch = l & 1;
        f4 t = {0, 0, 0, 0};
#pragma unroll
        for (int i = 0; i < 8; i++) t += __builtin_bit_cast(f4, lds_r128(RED + rg * 2048 + i * 256 + (l & 15) * 16));
        const int nn = 8 * (rg & 1) + r8;
        const int row = 32 * s + 8 * (nn >> 2) + 4 * (rg >> 1) + (nn & 3);
        const unsigned eo = row * 640 + j * 8 + ch * 4;
        u2 xv = *(const u2*)(x + eo);
        u2 bv = ld64(c.rs, eo * 2, OFF_B);
        float r0 = bfr(bflo(xv[0]) + bfr(t[0])) + bflo(bv[0]);
        float r1 = bfr(bfhi(xv[0]) + bfr(t[1])) + bfhi(bv[0]);
        float r2 = bfr(bflo(xv[1]) + bfr(t[2])) + bflo(bv[1]);
        float r3 = bfr(bfhi(xv[1]) + bfr(t[3])) + bfhi(bv[1]);
        u2 o = {pack2(r0, r1), pack2(r2, r3)};
        *(u2*)(out + eo) = o;
      }
      bar();
    }
  }
}

constexpr int IMG_G1 = 36864;
DEV void g1_dma(const Ctx& c, const u16* Wg, const u16* Wu, int cu, int kb, lchar* buf) {
#pragma unroll
  for (int i = 0; i < 9; i++) {
    const int slot = i * 256 + c.tid;
    const int row = slot / 18, cc = slot - row * 18;
    const int k = 64 * kb + swap13(row & 63);
    const u16* src = ((row >> 6) ? Wu : Wg) + (size_t)k * 1536 + cu * 128 + (cc < 16 ? cc : 15) * 8;
    dma16(src, buf + (i * 256 + c.w * 64) * 16, 0);
  }
}
DEV void role_G1(const Ctx& c, int cu, const u16* Wg, const u16* Wu) {
  u8v W[4][10];
  const int l = c.l, g = l >> 4, w = c.w;
  {
    const int q = (l >> 2) & 3, p = l & 3;
    const int lb = (16 * (g >> 1) + 4 * (g & 1) + q) * 288 + (32 * w + 4 * p) * 2;
#pragma unroll
    for (int i = 0; i < 4; i++) g1_dma(c, Wg, Wu, cu, i, c.L + i * IMG_G1);
#pragma unroll
    for (int kb = 0; kb < 10; kb++) {
      if (kb + 3 < 10) asm volatile("s_waitcnt vmcnt(27)" ::: "memory"); else wait_vm0();
      bar();
      lchar* bp = c.L + (kb & 3) * IMG_G1 + lb;
#pragma unroll
      for (int tt = 0; tt < 4; tt++) {
        const int o = (tt >> 1) * 64 * 288 + (tt & 1) * 32;
        u2 a = trread(bp + o), b = trread(bp + o + 8 * 288), cc = trread(bp + o + 32 * 288), d = trread(bp + o + 40 * 288);
        W[tt][kb] = (u8v){a[0], a[1], b[0], b[1], cc[0], cc[1], d[0], d[1]};
      }
      bar();
      if (kb + 4 < 10) g1_dma(c, Wg, Wu, cu, kb + 4, c.L + (kb & 3) * IMG_G1);
    }
  }
  lchar* XB = c.L;
  const char* xsrc = c.ws + OFF_XNP + c.tid * 16;
#pragma unroll
  for (int i = 0; i < 10; i++) dma16(xsrc + i * 4096, XB + i * 4096 + w * 1024, 0);
  for (int s = 0; s < NSEG; s++) {
    wait_vm0();
    bar();
    if (s > 0 && c.tid == 0) st32(1u, c.rs, FL_H + cu * 4, OFF_FLAG + (s - 1) * 2048);
    if (s + 1 < NSEG) {
#pragma unroll
      for (int i = 0; i < 10; i++) dma16(xsrc + (size_t)(s + 1) * 40960 + i * 4096, XB + ((s + 1) & 1) * 40960 + i * 4096 + w * 1024, 0);
    }
    lchar* xb = XB + (s & 1) * 40960 + l * 16;
    f4 acc[2][4];
#pragma unroll
    for (int t = 0; t < 4; t++) { acc[0][t] = (f4){0, 0, 0, 0}; acc[1][t] = (f4){0, 0, 0, 0}; }
#pragma unroll
    for (int kb = 0; kb < 10; kb++) {
      bf8 a00 = as_bf8(lds_r128(xb + kb * 2048)), a01 = as_bf8(lds_r128(xb + kb * 2048 + 1024));
      bf8 a10 = as_bf8(lds_r128(xb + 20480 + kb * 2048)), a11 = as_bf8(lds_r128(xb + 20480 + kb * 2048 + 1024));
#pragma unroll
      for (int t = 0; t < 4; t++) { acc[0][t] = mfma32(lo8(W[t][kb]), a00, acc[0][t]); acc[1][t] = mfma32(lo8(W[t][kb]), a10, acc[1][t]); }
#pragma unroll
      for (int t = 0; t < 4; t++) { acc[0][t] = mfma32(hi8(W[t][kb]), a01, acc[0][t]); acc[1][t] = mfma32(hi8(W[t][kb]), a11, acc[1][t]); }
    }
#pragma unroll
    for (int rt = 0; rt < 2; rt++) {
      float hv[8];
#pragma unroll
      for (int t = 0; t < 2; t++)
#pragma unroll
        for (int i = 0; i < 4; i++) {
          float gg = bfr(acc[rt][t][i]), uu = bfr(acc[rt][2 + t][i]);
          gg = gg * (gg > 0.f ? 1.f : 0.f);
          hv[4 * t + i] = gg * uu;
        }
      u4 o = {pack2(hv[0], hv[1]), pack2(hv[2], hv[3]), pack2(hv[4], hv[5]), pack2(hv[6], hv[7])};
      st128(o, c.rs, (4 * cu + w) * 1024 + l * 16, OFF_H + (s * 2 + rt) * 49152);
    }
  }
  wait_vm0();
  bar();
  if (c.tid == 0) st32(1u, c.rs, FL_H + cu * 4, OFF_FLAG + (NSEG - 1) * 2048);
}

constexpr int IMG_G2 = 40960;
DEV void g2_dma(const Ctx& c, const u16* Wd, int cu, int ch, lchar* buf) {
#pragma unroll
  for (int i = 0; i < 10; i++) {
    const int slot = i * 256 + c.tid;
    const int row = slot / 10, cc = slot - row * 10;
    const u16* src = Wd + (size_t)(256 * ch + row) * 640 + cu * 64 + (cc < 8 ? cc : 7) * 8;
    dma16(src, buf + (i * 256 + c.w * 64) * 16, 0);
  }
}
DEV void role_G2(const Ctx& c, int cu, const u16* Wd) {
  bool dead = false;
  u4 W[48];
  const int l = c.l, g = l >> 4, n = l & 15, w = c.w;
  {
    const int q = (l >> 2) & 3, p = l & 3;
    const int lb = (4 * g + q) * 160 + (16 * w + 4 * p) * 2;
#pragma unroll
    for (int i = 0; i < 3; i++) g2_dma(c, Wd, cu, i, c.L + i * IMG_G2);
#pragma unroll
    for (int ch = 0; ch < 6; ch++) {
      if (ch + 2 < 6) asm volatile("s_waitcnt vmcnt(20)" ::: "memory"); else wait_vm0();
      bar();
      lchar* bp = c.L + (ch % 3) * IMG_G2 + lb;
#pragma unroll
      for (int u = 0; u < 8; u++) {
        u2 a = trread(bp + u * 32 * 160), b = trread(bp + (u * 32 + 16) * 160);
        W[ch * 8 + u] = (u4){a[0], a[1], b[0], b[1]};
      }
      bar();
      if (ch + 3 < 6) g2_dma(c, Wd, cu, ch + 3, c.L + (ch % 3) * IMG_G2);
    }
  }
  lchar* HB = c.L;
  const char* hsrc = c.ws + OFF_H + c.tid * 16;
  const unsigned hfo = FL_H + (l < 12 ? l : 11) * 4;
  wait_flag(c.rs, hfo, OFF_FLAG, dead);
#pragma unroll
  for (int i = 0; i < 12; i++) dma16(hsrc + i * 4096, HB + i * 4096 + w * 1024, 1);
  for (int s = 0; s < NSEG; s++) {
#pragma unroll
    for (int rt = 0; rt < 2; rt++) {
      wait_vm0();
      bar();
      if (rt == 0 && s > 0 && c.tid == 0) st32(1u, c.rs, FL_B + cu * 4, OFF_FLAG + (s - 1) * 2048);
      const int nx = 2 * s + rt + 1;
      if (nx < 2 * NSEG) {
        if (rt == 1) wait_flag(c.rs, hfo, OFF_FLAG + (s + 1) * 2048, dead);
#pragma unroll
        for (int i = 0; i < 12; i++) dma16(hsrc + (size_t)nx * 49152 + i * 4096, HB + (rt ^ 1) * 49152 + i * 4096 + w * 1024, 1);
      }
      lchar* hb = HB + rt * 49152 + l * 16;
      f4 acc[4];
#pragma unroll
      for (int i = 0; i < 4; i++) acc[i] = (f4){0, 0, 0, 0};
#pragma unroll
      for (int u = 0; u < 48; u++) acc[u & 3] = mfma32(as_bf8(W[u]), as_bf8(lds_r128(hb + u * 1024)), acc[u & 3]);
      f4 t = (acc[0] + acc[1]) + (acc[2] + acc[3]);
      const int row = 32 * s + 8 * (n >> 2) + 4 * rt + (n & 3);
      u2 o = {pack2(t[0], t[1]), pack2(t[2], t[3])};
      st64(o, c.rs, (row * 640 + cu * 64 + w * 16 + 4 * g) * 2, OFF_B);
    }
  }
  wait_vm0();
  bar();
  if (c.tid == 0) st32(1u, c.rs, FL_B + cu * 4, OFF_FLAG + (NSEG - 1) * 2048);
}

__global__ void __launch_bounds__(256, 1) k_main(const u16* __restrict__ x, const u16* __restrict__ Wq, const u16* __restrict__ Wk,
                                                 const u16* __restrict__ Wg, const u16* __restrict__ Wu, const u16* __restrict__ Wd,
                                                 u16* __restrict__ out, char* __restrict__ ws) {
  __shared__ __attribute__((aligned(16))) char smem[163840];
  Ctx c;
  c.L = (lchar*)smem;
  c.ws = ws;
  c.rs = __builtin_amdgcn_make_buffer_rsrc(ws, 0, WS_USED, 0x00020000);
  c.tid = threadIdx.x;
  c.l = threadIdx.x & 63;
  c.w = __builtin_amdgcn_readfirstlane(threadIdx.x >> 6);
  const int b = blockIdx.x;
  if (b < 32) role_S(c, b, Wq, Wk);
  else if (b < 112) role_R(c, b - 32);
  else if (b < 144) role_X(c, b - 112, Wq, Wk);
  else if (b < 224) role_R2(c, b - 144, x, out);
  else if (b < 236) role_G1(c, b - 224, Wg, Wu);
  else if (b < 246) role_G2(c, b - 236, Wd);
}

extern "C" void kernel_launch(void* const* d_in, const int* in_sizes, int n_in, void* d_out, int out_size, void* d_ws, size_t ws_size,
                              hipStream_t stream) {
  const u16* x = (const u16*)d_in[0];
  const u16* mem0 = (const u16*)d_in[1];
  const u16* Wq = (const u16*)d_in[2];
  const u16* Wk = (const u16*)d_in[3];
  const u16* Wv = (const u16*)d_in[4];
  const u16* Wo = (const u16*)d_in[5];
  const u16* Wg = (const u16*)d_in[8];
  const u16* Wu = (const u16*)d_in[9];
  const u16* Wd = (const u16*)d_in[10];
  char* ws = (char*)d_ws;
  k_prep<<<640, 256, 0, stream>>>(x, mem0, ws);
  k_dgemm<<<800, 256, 0, stream>>>(Wv, Wo, ws);
  k_main<<<246, 256, 0, stream>>>(x, Wq, Wk, Wg, Wu, Wd, (u16*)d_out, ws);
}
```

```cpp
#include <hip/hip_runtime.h>
#include <stdint.h>

typedef __attribute__((ext_vector_type(2))) __bf16 bf2;
typedef __attribute__((ext_vector_type(4))) __bf16 bf4;
typedef __attribute__((ext_vector_type(8))) __bf16 bf8;
typedef __attribute__((ext_vector_type(16))) __bf16 bf16v;
typedef __attribute__((ext_vector_type(4))) float f4;
typedef __attribute__((ext_vector_type(2))) unsigned u2;
typedef __attribute__((ext_vector_type(4))) unsigned u4;
typedef __attribute__((ext_vector_type(8))) unsigned u8v;
typedef __attribute__((ext_vector_type(4))) short s4v;
typedef __attribute__((address_space(3))) char lchar;
typedef __attribute__((address_space(1))) const void gvoid;
typedef unsigned short u16;

#define DEV __device__ __forceinline__
#define CB() asm volatile("" ::: "memory")
#define SENT 0xFFFFFFFFu

constexpr int NSEG = 64;
constexpr unsigned MB = 1u << 20;
constexpr unsigned OFF_XNP = 0;
constexpr unsigned OFF_D = 4 * MB;
constexpr unsigned OFF_MEM = 32 * MB;
constexpr unsigned OFF_PM = 33 * MB;
constexpr unsigned OFF_FLAG = 35 * MB;
constexpr unsigned OFF_XNM = 36 * MB;
constexpr unsigned OFF_KX = 40 * MB;
constexpr unsigned OFF_QX = 64 * MB;
constexpr unsigned OFF_KM = 88 * MB;
constexpr unsigned OFF_UM = 100 * MB;
constexpr unsigned OFF_PX = 120 * MB;
constexpr unsigned OFF_H = 128 * MB;
constexpr unsigned OFF_B = 136 * MB;
constexpr unsigned OFF_UX = 140 * MB;
constexpr unsigned WS_USED = 220 * MB;
constexpr unsigned FL_KX = 0, FL_KM = 128, FL_PX = 256, FL_H = 512, FL_B = 640, FL_Z = 768;
constexpr int SPIN_MAX = 1 << 18;

DEV unsigned pack2(float a, float b) { bf2 v = {(__bf16)a, (__bf16)b}; return __builtin_bit_cast(unsigned, v); }
DEV float bfr(float a) { return (float)(__bf16)a; }
DEV float bflo(unsigned d) { return __builtin_bit_cast(float, d << 16); }
DEV float bfhi(unsigned d) { return __builtin_bit_cast(float, d & 0xFFFF0000u); }
DEV unsigned nosent(unsigned d) { return d < 0xFFFFFFFEu ? d : 0xFFFFFFFEu; }
DEV bf8 as_bf8(u4 v) { return __builtin_bit_cast(bf8, v); }
DEV bf8 lo8(const u8v& v) { u4 t = {v[0], v[1], v[2], v[3]}; return __builtin_bit_cast(bf8, t); }
DEV bf8 hi8(const u8v& v) { u4 t = {v[4], v[5], v[6], v[7]}; return __builtin_bit_cast(bf8, t); }
DEV f4 mfma32(bf8 a, bf8 b, f4 c) { return __builtin_amdgcn_mfma_f32_16x16x32_bf16(a, b, c, 0, 0, 0); }
DEV f4 mfma16(u2 a, u2 b, f4 c) {
  return __builtin_amdgcn_mfma_f32_16x16x16bf16_1k(__builtin_bit_cast(s4v, a), __builtin_bit_cast(s4v, b), c, 0, 0, 0);
}
DEV f4 smf(u4 a, const u8v& b, f4 c, int idx) {
  return __builtin_amdgcn_smfmac_f32_16x16x64_bf16(__builtin_bit_cast(bf8, a), __builtin_bit_cast(bf16v, b), c, idx, 0, 0);
}
DEV u2 trread(lchar* p) {
  bf4 v = __builtin_amdgcn_ds_read_tr16_b64_v4bf16((__attribute__((address_space(3))) bf4*)p);
  return __builtin_bit_cast(u2, v);
}
DEV u4 lds_r128(lchar* p) { return *(__attribute__((address_space(3))) u4*)p; }
DEV void lds_w128(lchar* p, u4 v) { *(__attribute__((address_space(3))) u4*)p = v; }
DEV void lds_w64(lchar* p, u2 v) { *(__attribute__((address_space(3))) u2*)p = v; }
DEV void gdma(const void* g, unsigned m0v) {
  asm volatile("s_mov_b32 m0, %1\n\ts_nop 0\n\tglobal_load_lds_dwordx4 %0, off" :: "v"(g), "s"(m0v) : "memory");
}
template <int IMM, int SC1> DEV void bdma(u4 srd, unsigned voff, unsigned soff, unsigned m0v) {
  if (SC1) asm volatile("s_mov_b32 m0, %3\n\ts_nop 0\n\tbuffer_load_dwordx4 %0, %1, %2 offen offset:%4 sc1 lds" :: "v"(voff), "s"(srd), "s"(soff), "s"(m0v), "i"(IMM) : "memory");
  else asm volatile("s_mov_b32 m0, %3\n\ts_nop 0\n\tbuffer_load_dwordx4 %0, %1, %2 offen offset:%4 lds" :: "v"(voff), "s"(srd), "s"(soff), "s"(m0v), "i"(IMM) : "memory");
}
DEV u8v toA(u8v v) { u8v r; asm("" : "=a"(r) : "0"(v)); return r; }
DEV u4 toA4(u4 v) { u4 r; asm("" : "=a"(r) : "0"(v)); return r; }
template <int N> DEV void wait_vm() { asm volatile("s_waitcnt vmcnt(%0)" :: "i"(N) : "memory"); }
typedef __amdgpu_buffer_rsrc_t rsrc_t;
DEV u4 ld128(rsrc_t r, unsigned v, unsigned s) { CB(); u4 x = __builtin_amdgcn_raw_buffer_load_b128(r, v, s, 16); CB(); return x; }
DEV u2 ld64(rsrc_t r, unsigned v, unsigned s) { CB(); u2 x = __builtin_amdgcn_raw_buffer_load_b64(r, v, s, 16); CB(); return x; }
DEV unsigned ld32(rsrc_t r, unsigned v, unsigned s) { CB(); unsigned x = __builtin_amdgcn_raw_buffer_load_b32(r, v, s, 16); CB(); return x; }
DEV void st128(u4 d, rsrc_t r, unsigned v, unsigned s) { __builtin_amdgcn_raw_buffer_store_b128(d, r, v, s, 16); }
DEV void st64(u2 d, rsrc_t r, unsigned v, unsigned s) { __builtin_amdgcn_raw_buffer_store_b64(d, r, v, s, 16); }
DEV void st32(unsigned d, rsrc_t r, unsigned v, unsigned s) { __builtin_amdgcn_raw_buffer_store_b32(d, r, v, s, 16); }
DEV void wait_vm0() { asm volatile("s_waitcnt vmcnt(0)" ::: "memory"); }
DEV void wait_lgkm0() { asm volatile("s_waitcnt lgkmcnt(0)" ::: "memory"); }
DEV void bar() { asm volatile("s_waitcnt lgkmcnt(0)\n\ts_barrier" ::: "memory"); }
DEV unsigned umax4(u4 v) { return max(max(v[0], v[1]), max(v[2], v[3])); }
DEV float u2f(unsigned u) { return __builtin_bit_cast(float, u); }
DEV unsigned f2u(float f) { return __builtin_bit_cast(unsigned, f); }
DEV float xmax32(float v) { u2 r = __builtin_amdgcn_permlane32_swap(f2u(v), f2u(v), false, false); unsigned a = r[0], b = r[1]; return fmaxf(u2f(a), u2f(b)); }
DEV float xmax16(float v) { u2 r = __builtin_amdgcn_permlane16_swap(f2u(v), f2u(v), false, false); unsigned a = r[0], b = r[1]; return fmaxf(u2f(a), u2f(b)); }
DEV float xsum32(float v) { u2 r = __builtin_amdgcn_permlane32_swap(f2u(v), f2u(v), false, false); unsigned a = r[0], b = r[1]; return u2f(a) + u2f(b); }
DEV float xsum16(float v) { u2 r = __builtin_amdgcn_permlane16_swap(f2u(v), f2u(v), false, false); unsigned a = r[0], b = r[1]; return u2f(a) + u2f(b); }
DEV float halfsum(float a, float b) {
  u2 r = __builtin_amdgcn_permlane32_swap(f2u(a), f2u(b), false, false);
  unsigned x = r[0], y = r[1];
  return u2f(x) + u2f(y);
}

DEV void wait_flag(rsrc_t r, unsigned voff, unsigned soff, bool& dead) {
  if (dead) return;
  for (int i = 0; i < SPIN_MAX; ++i) {
    unsigned f = ld32(r, voff, soff);
    if (__all(f != 0)) return;
    __builtin_amdgcn_s_sleep(1);
  }
  dead = true;
}

__global__ void __launch_bounds__(256) k_prep(const u16* __restrict__ x, const u16* __restrict__ mem0, char* __restrict__ ws) {
  const int tid = threadIdx.x, l = tid & 63, w = tid >> 6;
  const int b = blockIdx.x;
  if (b < 512) {
    const int row = b * 4 + w;
    u4 d0 = {0, 0, 0, 0}, d1 = {0, 0, 0, 0};
    const bool act = l < 40;
    if (act) {
      const u4* p = (const u4*)(x + (size_t)row * 640 + l * 16);
      d0 = p[0]; d1 = p[1];
    }
    float v[16];
#pragma unroll
    for (int i = 0; i < 4; i++) { v[2 * i] = bflo(d0[i]); v[2 * i + 1] = bfhi(d0[i]); v[8 + 2 * i] = bflo(d1[i]); v[8 + 2 * i + 1] = bfhi(d1[i]); }
    float s = 0;
#pragma unroll
    for (int i = 0; i < 16; i++) s += v[i];
#pragma unroll
    for (int o = 32; o >= 1; o >>= 1) s += __shfl_xor(s, o);
    const float mean = s * (1.0f / 640.0f);
    float q = 0;
    if (act) {
#pragma unroll
      for (int i = 0; i < 16; i++) { float d = v[i] - mean; q += d * d; }
    }
#pragma unroll
    for (int o = 32; o >= 1; o >>= 1) q += __shfl_xor(q, o);
    const float mb = bfr(mean);
    const float vb = bfr(q * (1.0f / 640.0f));
    const float ve = bfr(vb + 1e-5f);
    const float sd = bfr(sqrtf(ve));
    if (act) {
      unsigned o[8];
#pragma unroll
      for (int i = 0; i < 8; i++) o[i] = pack2(bfr(v[2 * i] - mb) / sd, bfr(v[2 * i + 1] - mb) / sd);
      const int seg = row >> 5, rr = row & 31;
      const int n = 8 * (rr >> 4) + (rr & 7), rt = (rr >> 3) & 1;
      const int kb32 = l >> 1, g0 = 2 * (l & 1);
      char* dst = ws + OFF_XNP + (size_t)(((seg * 2 + rt) * 20 + kb32) * 1024) + (g0 * 16 + n) * 16;
      u4 c0 = {o[0], o[4], o[1], o[5]}, c1 = {o[2], o[6], o[3], o[7]};
      *(u4*)dst = c0;
      *(u4*)(dst + 256) = c1;
      char* dm = ws + OFF_XNM + (size_t)(row >> 3) * 10240 + l * 256 + (row & 7) * 16;
      *(u4*)dm = (u4){o[0], o[1], o[2], o[3]};
      *(u4*)(dm + 128) = (u4){o[4], o[5], o[6], o[7]};
    }
  } else {
    const int fb = b - 512;
    const unsigned gt = fb * 256 + tid;
    const u4 sv = {SENT, SENT, SENT, SENT}, zv = {0, 0, 0, 0};
    for (unsigned i = gt; i < (63 * 10240) / 16; i += 32768) *(u4*)(ws + OFF_MEM + 10240 + (size_t)i * 16) = sv;
    for (unsigned i = gt; i < (64 * 20480) / 16; i += 32768) *(u4*)(ws + OFF_PM + (size_t)i * 16) = sv;
    for (unsigned i = gt; i < (64 * 2048) / 16; i += 32768) *(u4*)(ws + OFF_FLAG + (size_t)i * 16) = zv;
    if (gt < 640) {
      const int j = gt >> 3, r = gt & 7;
      u4 d = *(const u4*)(mem0 + r * 640 + j * 8);
      d[0] = nosent(d[0]); d[1] = nosent(d[1]); d[2] = nosent(d[2]); d[3] = nosent(d[3]);
      *(u4*)(ws + OFF_MEM + gt * 16) = d;
    }
  }
}

__global__ void __launch_bounds__(256) k_dgemm(const u16* __restrict__ Wv, const u16* __restrict__ Wo, char* __restrict__ ws) {
  __shared__ __attribute__((aligned(16))) char smem[192 * 256];
  lchar* L = (lchar*)smem;
  const int tid = threadIdx.x, l = tid & 63, w = __builtin_amdgcn_readfirstlane(tid >> 6);
  const int b = blockIdx.x;
  const int h = b / 25, mt = (b % 25) / 5, nt5 = b % 5;
#pragma unroll
  for (int i = 0; i < 12; i++) {
    const int slot = i * 256 + tid, r = slot >> 4, c16 = slot & 15;
    u4 d = *(const u4*)(Wo + ((size_t)h * 192 + r) * 640 + nt5 * 128 + c16 * 8);
    const int f = (r & 3) | ((r >> 1) & 4);
    lds_w128(L + r * 256 + (((c16 >> 1) ^ f) * 32) + (c16 & 1) * 16, d);
  }
  const int m = l & 15, g = l >> 4;
  const int kb = mt * 2 + (w >> 1), half = w & 1;
  const int mg = m >> 2, mi = m & 3;
  const int din = 64 * kb + 32 * half + 16 * (mg >> 1) + 4 * (mg & 1) + 8 * (mi >> 1) + (mi & 1);
  const u16* pa = Wv + (size_t)din * 6144 + h * 192 + g * 8;
  u4 A[6][2];
#pragma unroll
  for (int u = 0; u < 6; u++) { A[u][0] = *(const u4*)(pa + u * 32); A[u][1] = *(const u4*)(pa + 2 * 6144 + u * 32); }
  __syncthreads();
  f4 acc[2][8];
#pragma unroll
  for (int i = 0; i < 8; i++) { acc[0][i] = (f4){0, 0, 0, 0}; acc[1][i] = (f4){0, 0, 0, 0}; }
  const int q = (l >> 2) & 3, p = l & 3;
#pragma unroll
  for (int u = 0; u < 6; u++) {
#pragma unroll
    for (int nt = 0; nt < 8; nt++) {
      const int r0 = 32 * u + 8 * g + q, r1 = r0 + 4;
      const int f = q | ((r0 >> 1) & 4);
      u2 b0 = trread(L + r0 * 256 + ((nt ^ f) * 32) + p * 8);
      u2 b1 = trread(L + r1 * 256 + ((nt ^ f) * 32) + p * 8);
      u4 B = {b0[0], b0[1], b1[0], b1[1]};
      acc[0][nt] = mfma32(as_bf8(A[u][0]), as_bf8(B), acc[0][nt]);
      acc[1][nt] = mfma32(as_bf8(A[u][1]), as_bf8(B), acc[1][nt]);
    }
  }
  const int hsel = h >> 4, hh = h & 15;
#pragma unroll
  for (int nt = 0; nt < 8; nt++) {
    const int col = nt5 * 128 + nt * 16 + m;
    const int j = col >> 3, c = col & 7;
    u4 o = {pack2(acc[0][nt][0], acc[0][nt][1]), pack2(acc[0][nt][2], acc[0][nt][3]), pack2(acc[1][nt][0], acc[1][nt][1]), pack2(acc[1][nt][2], acc[1][nt][3])};
    size_t off = (size_t)j * 327680 + (size_t)(((hh * 10 + kb) * 2 + half) * 1024) + (hsel * 8 + c + 16 * g) * 16;
    *(u4*)(ws + OFF_D + off) = o;
  }
}

#define TS(step, slot)
struct Ctx { lchar* L; char* ws; rsrc_t rs; u4 srd; unsigned lb; int tid, l, w; };

DEV int swap13(int r) { return (r & ~10) | ((r >> 2) & 2) | ((r << 2) & 8); }

template <int NK, int SC1> DEV void lin_dma(const Ctx& c, unsigned soff, unsigned ldsoff) {
  const unsigned v = c.w * (NK * 1024) + c.l * 16;
  const unsigned m = c.lb + ldsoff + c.w * (NK * 1024);
#pragma unroll
  for (int i = 0; i < NK; i += 4) {
    bdma<0, SC1>(c.srd, v, soff + i * 1024, m + i * 1024);
    if (i + 1 < NK) bdma<1024, SC1>(c.srd, v, soff + i * 1024, m + i * 1024);
    if (i + 2 < NK) bdma<2048, SC1>(c.srd, v, soff + i * 1024, m + i * 1024);
    if (i + 3 < NK) bdma<3072, SC1>(c.srd, v, soff + i * 1024, m + i * 1024);
  }
}

DEV void gather_mem(const Ctx& c, int s, lchar* MX, bool& dead, u4 (&A)[10]) {
  const unsigned so = OFF_MEM + s * 10240;
  u4 a0, a1; u2 a2;
  int it = 0;
  while (true) {
    a0 = ld128(c.rs, c.tid * 16, so);
    a1 = ld128(c.rs, 4096 + c.tid * 16, so);
    a2 = ld64(c.rs, 8192 + c.tid * 8, so);
    unsigned mx = max(max(umax4(a0), umax4(a1)), max(a2[0], a2[1]));
    if (__all(mx != SENT) || dead) break;
    if (++it > SPIN_MAX) { dead = true; break; }
  }
  lds_w128(MX + c.tid * 16, a0);
  lds_w128(MX + 4096 + c.tid * 16, a1);
  lds_w64(MX + 8192 + c.tid * 8, a2);
  bar();
#pragma unroll
  for (int i = 0; i < 10; i++) A[i] = lds_r128(MX + i * 1024 + c.l * 16);
}

constexpr int IMG_QK = 53248;
DEV void qk_dma(const Ctx& c, const u16* Wq, const u16* Wk, int h, int kb, unsigned buf) {
#pragma unroll
  for (int i = 0; i < 13; i++) {
    const int slot = i * 256 + c.tid;
    const int row = slot / 26, cc = slot - row * 26;
    const int k = 64 * kb + swap13(row & 63);
    const u16* src = ((row >> 6) ? Wk : Wq) + (size_t)k * 6144 + h * 192 + (cc < 24 ? cc : 23) * 8;
    gdma(src, c.lb + buf + (i * 256 + c.w * 64) * 16);
  }
}
DEV u8v qk_frag(lchar* lb, int tt) {
  const int o = (tt / 3) * 64 * 416 + (tt % 3) * 32;
  u2 a = trread(lb + o), b = trread(lb + o + 8 * 416), cc = trread(lb + o + 32 * 416), d = trread(lb + o + 40 * 416);
  u8v r = {a[0], a[1], b[0], b[1], cc[0], cc[1], d[0], d[1]};
  return r;
}
DEV int qk_lanebase(int l, int w) {
  const int gb = l >> 4, q = (l >> 2) & 3, p = l & 3;
  return (16 * (gb >> 1) + 4 * (gb & 1) + q) * 416 + (48 * w + 4 * p) * 2;
}
DEV void qk_load(const Ctx& c, const u16* Wq, const u16* Wk, int h, u8v (&W)[6][8]) {
  const int lb = qk_lanebase(c.l, c.w);
  qk_dma(c, Wq, Wk, h, 0, 0);
  qk_dma(c, Wq, Wk, h, 1, IMG_QK);
  qk_dma(c, Wq, Wk, h, 2, 2 * IMG_QK);
#pragma unroll
  for (int kb = 0; kb < 8; kb++) {
    wait_vm<26>();
    bar();
#pragma unroll
    for (int tt = 0; tt < 6; tt++) {
      u8v f = qk_frag(c.L + (kb % 3) * IMG_QK + lb, tt);
      W[tt][kb] = (kb * 6 + tt < 28) ? toA(f) : f;
    }
    bar();
    if (kb + 3 < 10) qk_dma(c, Wq, Wk, h, kb + 3, (kb % 3) * IMG_QK);
  }
  wait_vm0();
  bar();
}

constexpr float SCALE = 0.07216878364870322f;

DEV void role_S(const Ctx& c, int h, const u16* Wq, const u16* Wk) {
  bool dead = false;
  u8v W[6][8];
  qk_load(c, Wq, Wk, h, W);
  constexpr unsigned KXO = IMG_QK;
  lchar* KXB = c.L + KXO;
  lchar* QKI = KXB + 24576;
  lchar* MX = QKI + 6144;
  lchar* w8 = c.L + 2 * IMG_QK + qk_lanebase(c.l, c.w);
  lchar* w9 = c.L + qk_lanebase(c.l, c.w);
  const int l = c.l, g = l >> 4, n = l & 15;
  const int idx = (l & 8) ? 0xEEEE : 0x4444;
  const unsigned kxv = (c.w - 1) * 4096 + l * 16;
  const unsigned kxm = c.lb + KXO + (c.w - 1) * 4096;
  if (c.w > 0) {
    wait_flag(c.rs, FL_KX + h * 4, OFF_FLAG, dead);
    const unsigned so = OFF_KX + h * 12288;
    bdma<0, 1>(c.srd, kxv, so, kxm); bdma<1024, 1>(c.srd, kxv, so, kxm); bdma<2048, 1>(c.srd, kxv, so, kxm); bdma<3072, 1>(c.srd, kxv, so, kxm);
  }
  for (int s = 0; s < NSEG; s++) {
    u4 A[10];
    TS(s, 0);
    gather_mem(c, s, MX, dead, A);
    TS(s, 1);
    f4 acc[6];
#pragma unroll
    for (int tt = 0; tt < 6; tt++) acc[tt] = (f4){0, 0, 0, 0};
    u8v f[3];
#pragma unroll
    for (int t = 0; t < 3; t++) f[t] = qk_frag(w8, t);
#pragma unroll
    for (int kb = 0; kb < 2; kb++)
#pragma unroll
      for (int tt = 0; tt < 6; tt++) acc[tt] = smf(A[kb], W[tt][kb], acc[tt], idx);
#pragma unroll
    for (int t = 0; t < 3; t++) acc[t] = smf(A[8], f[t], acc[t], idx);
#pragma unroll
    for (int t = 0; t < 3; t++) f[t] = qk_frag(w8, 3 + t);
#pragma unroll
    for (int kb = 2; kb < 4; kb++)
#pragma unroll
      for (int tt = 0; tt < 6; tt++) acc[tt] = smf(A[kb], W[tt][kb], acc[tt], idx);
#pragma unroll
    for (int t = 0; t < 3; t++) acc[3 + t] = smf(A[8], f[t], acc[3 + t], idx);
#pragma unroll
    for (int t = 0; t < 3; t++) f[t] = qk_frag(w9, t);
#pragma unroll
    for (int kb = 4; kb < 6; kb++)
#pragma unroll
      for (int tt = 0; tt < 6; tt++) acc[tt] = smf(A[kb], W[tt][kb], acc[tt], idx);
#pragma unroll
    for (int t = 0; t < 3; t++) acc[t] = smf(A[9], f[t], acc[t], idx);
#pragma unroll
    for (int t = 0; t < 3; t++) f[t] = qk_frag(w9, 3 + t);
#pragma unroll
    for (int kb = 6; kb < 8; kb++)
#pragma unroll
      for (int tt = 0; tt < 6; tt++) acc[tt] = smf(A[kb], W[tt][kb], acc[tt], idx);
#pragma unroll
    for (int t = 0; t < 3; t++) acc[3 + t] = smf(A[9], f[t], acc[3 + t], idx);
    u4 KX[12];
    if (c.w == 0) {
      lchar* kx = KXB + (s & 1) * 12288 + l * 16;
#pragma unroll
      for (int u = 0; u < 12; u++) KX[u] = lds_r128(kx + u * 1024);
    }
#pragma unroll
    for (int t = 0; t < 3; t++) {
      float v0 = halfsum(acc[t][0], acc[t + 3][0]), v1 = halfsum(acc[t][1], acc[t + 3][1]);
      float v2 = halfsum(acc[t][2], acc[t + 3][2]), v3 = halfsum(acc[t][3], acc[t + 3][3]);
      u2 o = {pack2(v0, v1), pack2(v2, v3)};
      lds_w64(QKI + (48 * c.w + 16 * t + n) * 32 + g * 8, o);
    }
    TS(s, 2);
    bar();
    TS(s, 3);
    if (c.w == 0) {
      u4 F[6];
      lchar* qb = QKI + (4 * g + ((l >> 2) & 3)) * 32 + (l & 3) * 8;
#pragma unroll
      for (int u = 0; u < 6; u++) { u2 a = trread(qb + u * 1024), b = trread(qb + u * 1024 + 512); F[u] = (u4){a[0], a[1], b[0], b[1]}; }
      if (s < NSEG - 1) {
        f4 T0 = {0, 0, 0, 0}, T1 = T0, T2 = T0;
#pragma unroll
        for (int u = 0; u < 6; u++) {
          T0 = mfma32(as_bf8(F[u]), as_bf8(F[u]), T0);
          T1 = mfma32(as_bf8(KX[u]), as_bf8(F[u]), T1);
          T2 = mfma32(as_bf8(KX[6 + u]), as_bf8(F[u]), T2);
        }
        float v[12];
#pragma unroll
        for (int i = 0; i < 4; i++) {
          v[i] = g >= 2 ? bfr(bfr(T0[i]) * SCALE) : -3.0e38f;
          v[4 + i] = bfr(bfr(T1[i]) * SCALE);
          v[8 + i] = bfr(bfr(T2[i]) * SCALE);
        }
        float mx = v[0];
#pragma unroll
        for (int i = 1; i < 12; i++) mx = fmaxf(mx, v[i]);
        mx = xmax32(xmax16(mx));
        float sum = 0;
#pragma unroll
        for (int i = 0; i < 12; i++) { v[i] = __expf(v[i] - mx); sum += v[i]; }
        sum = xsum32(xsum16(sum));
        const float inv = __builtin_amdgcn_rcpf(sum);
        u4 px = {nosent(pack2(v[4] * inv, v[5] * inv)), nosent(pack2(v[6] * inv, v[7] * inv)), nosent(pack2(v[8] * inv, v[9] * inv)), nosent(pack2(v[10] * inv, v[11] * inv))};
        u2 pm = {nosent(pack2(v[0] * inv, v[1] * inv)), nosent(pack2(v[2] * inv, v[3] * inv))};
        const unsigned so = OFF_PM + s * 20480 + h * 640;
        if (n < 8) {
          st128(px, c.rs, g * 128 + n * 16, so);
          if (g >= 2) st64(pm, c.rs, 512 + (g - 2) * 64 + n * 8, so);
        }
      }
      TS(s, 4);
      const unsigned ko = OFF_KM + (s * 32 + h) * 6144;
#pragma unroll
      for (int u = 0; u < 6; u++) st128(F[u], c.rs, u * 1024 + l * 16, ko);
      wait_vm0();
      if (l == 0) st32(1u, c.rs, FL_KM + h * 4, OFF_FLAG + s * 2048);
    } else if (s + 1 < NSEG - 1) {
      wait_flag(c.rs, FL_KX + h * 4, OFF_FLAG + (s + 1) * 2048, dead);
      const unsigned so = OFF_KX + ((s + 1) * 32 + h) * 12288, m = kxm + ((s + 1) & 1) * 12288;
      bdma<0, 1>(c.srd, kxv, so, m); bdma<1024, 1>(c.srd, kxv, so, m); bdma<2048, 1>(c.srd, kxv, so, m); bdma<3072, 1>(c.srd, kxv, so, m);
    }
  }
}

DEV void d_load(const Ctx& c, int j, u8v (&D)[4][10]) {
  const char* src = c.ws + OFF_D + (size_t)j * 327680 + (size_t)c.w * 81920 + c.l * 16;
#pragma unroll
  for (int t = 0; t < 4; t++)
#pragma unroll
    for (int kb = 0; kb < 10; kb++) {
      u4 a = *(const u4*)(src + (t * 10 + kb) * 2048), b = *(const u4*)(src + (t * 10 + kb) * 2048 + 1024);
      u8v f = (u8v){a[0], a[1], a[2], a[3], b[0], b[1], b[2], b[3]};
      D[t][kb] = (kb < 7) ? toA(f) : f;
    }
}
DEV void z_mm(const u4 (&A)[10], const u8v (&D)[4][10], int idx, u2 (&Z)[2]) {
  f4 acc[4];
#pragma unroll
  for (int t = 0; t < 4; t++) acc[t] = (f4){0, 0, 0, 0};
#pragma unroll
  for (int kb = 0; kb < 10; kb++)
#pragma unroll
    for (int t = 0; t < 4; t++) acc[t] = smf(A[kb], D[t][kb], acc[t], idx);
#pragma unroll
  for (int pp = 0; pp < 2; pp++) {
    float v0 = halfsum(acc[2 * pp][0], acc[2 * pp + 1][0]), v1 = halfsum(acc[2 * pp][1], acc[2 * pp + 1][1]);
    float v2 = halfsum(acc[2 * pp][2], acc[2 * pp + 1][2]), v3 = halfsum(acc[2 * pp][3], acc[2 * pp + 1][3]);
    Z[pp] = (u2){pack2(v0, v1), pack2(v2, v3)};
  }
}
DEV void z_pair(lchar* xb, const u8v (&D)[4][10], int idx, u4 (&UX)[2]) {
  u4 A[10]; u2 Z0[2], Z1[2];
#pragma unroll
  for (int i = 0; i < 10; i++) A[i] = lds_r128(xb + i * 1024);
  z_mm(A, D, idx, Z0);
#pragma unroll
  for (int i = 0; i < 10; i++) A[i] = lds_r128(xb + 10240 + i * 1024);
  z_mm(A, D, idx, Z1);
#pragma unroll
  for (int pp = 0; pp < 2; pp++) UX[pp] = (u4){Z0[pp][0], Z0[pp][1], Z1[pp][0], Z1[pp][1]};
}

DEV void role_R(const Ctx& c, int j) {
  bool dead = false;
  constexpr unsigned XBO = 12288;
  lin_dma<5, 0>(c, OFF_XNM, XBO);
  lin_dma<5, 0>(c, OFF_XNM + 20480, XBO + 20480);
  u8v D[4][10];
  d_load(c, j, D);
  lchar* MX = c.L;
  lchar* RED = c.L + 10240;
  lchar* xb0 = c.L + XBO + c.l * 16;
  lchar* xb1 = xb0 + 20480;
  const int l = c.l, g = l >> 4, n = l & 15, w = c.w;
  const int idx = (l & 8) ? 0xEEEE : 0x4444;
  const unsigned hb = (4 * w + (g >> 1) + 16 * (n >> 3)) * 640;
  const unsigned pxo = hb + (g & 1) * 128 + (n & 7) * 16;
  const unsigned pmo = hb + 512 + (g & 1) * 64 + (n & 7) * 8;
  const unsigned uxo = j * 16384 + w * 4096 + l * 16;
  wait_vm0();
  bar();
  u4 U0[2], U1[2];
  z_pair(xb0, D, idx, U0);
  for (int s = 0; s < NSEG; s++) {
    u4 A[10];
    TS(s, 0);
    gather_mem(c, s, MX, dead, A);
    TS(s, 1);
    if (s > 0 && c.tid == 64) st32(1u, c.rs, FL_Z + j * 4, OFF_FLAG + (s - 1) * 2048);
    if (s + 1 < NSEG) lin_dma<5, 0>(c, OFF_XNM + (s + 1) * 40960, XBO);
    u2 ZM[2];
    z_mm(A, D, idx, ZM);
    z_pair(xb1, D, idx, U1);
    if (s < NSEG - 1) {
      const unsigned so = OFF_PM + s * 20480;
      TS(s, 2);
      u4 P[4]; u2 PM[2];
      int it = 0;
      while (true) {
#pragma unroll
        for (int t = 0; t < 4; t++) P[t] = ld128(c.rs, pxo + (t >> 1) * 1280 + (t & 1) * 256, so);
#pragma unroll
        for (int pp = 0; pp < 2; pp++) PM[pp] = ld64(c.rs, pmo + pp * 1280, so);
        unsigned mx = max(max(umax4(P[0]), umax4(P[1])), max(umax4(P[2]), umax4(P[3])));
        mx = max(mx, max(max(PM[0][0], PM[0][1]), max(PM[1][0], PM[1][1])));
        if (__all(mx != SENT) || dead) break;
        if (++it > SPIN_MAX) { dead = true; break; }
      }
      TS(s, 3);
      f4 o0 = {0, 0, 0, 0}, o1 = o0;
      o0 = mfma32(as_bf8(U0[0]), as_bf8(P[0]), o0);
      o1 = mfma32(as_bf8(U1[0]), as_bf8(P[1]), o1);
      o0 = mfma32(as_bf8(U0[1]), as_bf8(P[2]), o0);
      o1 = mfma32(as_bf8(U1[1]), as_bf8(P[3]), o1);
      o0 = mfma16(ZM[0], PM[0], o0);
      o1 = mfma16(ZM[1], PM[1], o1);
      o0 += o1;
      if ((n >> 3) == (g >> 1)) lds_w128(RED + w * 512 + (g >> 1) * 256 + (n & 7) * 32 + (g & 1) * 16, __builtin_bit_cast(u4, o0));
      bar();
      if (w == 0 && l < 16) {
        f4 t = {0, 0, 0, 0};
#pragma unroll
        for (int i = 0; i < 8; i++) t += __builtin_bit_cast(f4, lds_r128(RED + i * 256 + l * 16));
        u2 o = {nosent(pack2(t[0], t[1])), nosent(pack2(t[2], t[3]))};
        st64(o, c.rs, j * 128 + l * 8, OFF_MEM + (s + 1) * 10240);
      }
      TS(s, 4);
    }
    {
      const unsigned uo = OFF_UX + s * (80 * 16384);
      st128(U0[0], c.rs, uxo, uo); st128(U1[0], c.rs, uxo + 1024, uo); st128(U0[1], c.rs, uxo + 2048, uo); st128(U1[1], c.rs, uxo + 3072, uo);
    }
#pragma unroll
    for (int pp = 0; pp < 2; pp++) st64(ZM[pp], c.rs, (w * 2 + pp) * 512 + l * 8, OFF_UM + (s * 80 + j) * 4096);
    if (s < NSEG - 1) {
      lin_dma<5, 0>(c, OFF_XNM + (s + 1) * 40960 + 20480, XBO + 20480);
      z_pair(xb0, D, idx, U0);
    } else {
      wait_vm0();
      bar();
      if (c.tid == 0) st32(1u, c.rs, FL_Z + j * 4, OFF_FLAG + s * 2048);
    }
  }
}

DEV void role_X(const Ctx& c, int h, const u16* Wq, const u16* Wk) {
  u8v W[6][8];
  qk_load(c, Wq, Wk, h, W);
  constexpr unsigned XBO = IMG_QK;
  lchar* w8 = c.L + 2 * IMG_QK + qk_lanebase(c.l, c.w);
  lchar* w9 = c.L + qk_lanebase(c.l, c.w);
  const int l = c.l, w = c.w;
  lin_dma<5, 0>(c, OFF_XNP, XBO);
  wait_vm0();
  for (int s = 0; s < NSEG; s++) {
#pragma unroll
    for (int rt = 0; rt < 2; rt++) {
      TS(s, rt * 2);
      wait_vm<6>();
      bar();
      TS(s, rt * 2 + 1);
      if (rt == 1 && s > 0 && c.tid == 0) st32(1u, c.rs, FL_KX + h * 4, OFF_FLAG + (s - 1) * 2048);
      const int nx = s * 2 + rt + 1;
      if (nx < 2 * NSEG) lin_dma<5, 0>(c, OFF_XNP + nx * 20480, XBO + (rt ^ 1) * 20480);
      lchar* xb = c.L + XBO + rt * 20480 + l * 16;
      f4 acc[6];
#pragma unroll
      for (int tt = 0; tt < 6; tt++) acc[tt] = (f4){0, 0, 0, 0};
      u4 b0 = lds_r128(xb), b1 = lds_r128(xb + 1024);
      u4 c80 = lds_r128(xb + 8 * 2048), c81 = lds_r128(xb + 8 * 2048 + 1024);
      u4 c90 = lds_r128(xb + 9 * 2048), c91 = lds_r128(xb + 9 * 2048 + 1024);
      u8v f[3];
#pragma unroll
      for (int kb = 0; kb < 8; kb++) {
        u4 n0, n1;
        if (kb < 7) { n0 = lds_r128(xb + (kb + 1) * 2048); n1 = lds_r128(xb + (kb + 1) * 2048 + 1024); }
        if ((kb & 1) == 0) {
#pragma unroll
          for (int t = 0; t < 3; t++) f[t] = qk_frag(kb < 4 ? w8 : w9, ((kb >> 1) & 1) * 3 + t);
        }
#pragma unroll
        for (int tt = 0; tt < 6; tt++) acc[tt] = mfma32(lo8(W[tt][kb]), as_bf8(b0), acc[tt]);
#pragma unroll
        for (int tt = 0; tt < 6; tt++) acc[tt] = mfma32(hi8(W[tt][kb]), as_bf8(b1), acc[tt]);
        if (kb & 1) {
          const int o = ((kb >> 1) & 1) * 3;
#pragma unroll
          for (int t = 0; t < 3; t++) acc[o + t] = mfma32(lo8(f[t]), as_bf8(kb < 4 ? c80 : c90), acc[o + t]);
#pragma unroll
          for (int t = 0; t < 3; t++) acc[o + t] = mfma32(hi8(f[t]), as_bf8(kb < 4 ? c81 : c91), acc[o + t]);
        }
        if (kb < 7) { b0 = n0; b1 = n1; }
      }
#pragma unroll
      for (int tt = 0; tt < 6; tt++) {
        const int t = 3 * w + tt % 3;
        u2 o = {pack2(acc[tt][0], acc[tt][1]), pack2(acc[tt][2], acc[tt][3])};
        st64(o, c.rs, rt * 6144 + (t >> 1) * 1024 + l * 16 + (t & 1) * 8, (tt < 3 ? OFF_QX : OFF_KX) + (s * 32 + h) * 12288);
      }
    }
  }
  wait_vm0();
  bar();
  if (c.tid == 0) st32(1u, c.rs, FL_KX + h * 4, OFF_FLAG + (NSEG - 1) * 2048);
}

DEV void role_A(const Ctx& c, int a) {
  bool dead = false;
  const int l = c.l, g = l >> 4, n = l & 15;
  const int h = 2 * a + (c.w >> 1), rt = c.w & 1;
  const int qi = 16 * (n >> 3) + 8 * rt + (n & 7) - 16 * (g >> 1) - 4 * (g & 1);
  for (int s = 0; s < NSEG; s++) {
    const unsigned fo = OFF_FLAG + s * 2048;
    TS(s, 0);
    wait_flag(c.rs, FL_KX + h * 4, fo, dead);
    const unsigned qo = OFF_QX + (s * 32 + h) * 12288 + rt * 6144, ko = OFF_KX + (s * 32 + h) * 12288, mo = OFF_KM + (s * 32 + h) * 6144;
    u4 Q[6], K0[6], K1[6], K2[6];
#pragma unroll
    for (int u = 0; u < 6; u++) { Q[u] = ld128(c.rs, u * 1024 + l * 16, qo); K1[u] = ld128(c.rs, u * 1024 + l * 16, ko); K2[u] = ld128(c.rs, 6144 + u * 1024 + l * 16, ko); }
    f4 T0 = {0, 0, 0, 0}, T1 = T0, T2 = T0;
#pragma unroll
    for (int u = 0; u < 6; u++) { T1 = mfma32(as_bf8(K1[u]), as_bf8(Q[u]), T1); T2 = mfma32(as_bf8(K2[u]), as_bf8(Q[u]), T2); }
    TS(s, 1);
    wait_flag(c.rs, FL_KM + h * 4, fo, dead);
    TS(s, 2);
#pragma unroll
    for (int u = 0; u < 6; u++) K0[u] = ld128(c.rs, u * 1024 + l * 16, mo);
#pragma unroll
    for (int u = 0; u < 6; u++) T0 = mfma32(as_bf8(K0[u]), as_bf8(Q[u]), T0);
    float v[12];
#pragma unroll
    for (int i = 0; i < 4; i++) {
      v[i] = g >= 2 ? bfr(bfr(T0[i]) * SCALE) : -3.0e38f;
      v[4 + i] = (i <= qi) ? bfr(bfr(T1[i]) * SCALE) : -3.0e38f;
      v[8 + i] = (8 + i <= qi) ? bfr(bfr(T2[i]) * SCALE) : -3.0e38f;
    }
    float mx = v[0];
#pragma unroll
    for (int i = 1; i < 12; i++) mx = fmaxf(mx, v[i]);
    mx = xmax32(xmax16(mx));
    float sum = 0;
#pragma unroll
    for (int i = 0; i < 12; i++) { v[i] = __expf(v[i] - mx); sum += v[i]; }
    sum = xsum32(xsum16(sum));
    const float inv = __builtin_amdgcn_rcpf(sum);
    u4 px = {pack2(v[4] * inv, v[5] * inv), pack2(v[6] * inv, v[7] * inv), pack2(v[8] * inv, v[9] * inv), pack2(v[10] * inv, v[11] * inv)};
    u2 pm = {pack2(v[0] * inv, v[1] * inv), pack2(v[2] * inv, v[3] * inv)};
    const unsigned so = OFF_PX + (s * 32 + h) * 2560 + rt * 1280;
    st128(px, c.rs, g * 256 + n * 16, so);
    if (g >= 2) st64(pm, c.rs, 1024 + (g - 2) * 128 + n * 8, so);
    wait_vm0();
    if (l == 0) st32(1u, c.rs, FL_PX + (h * 2 + rt) * 4, fo);
    TS(s, 3);
  }
}

DEV void role_O(const Ctx& c, int o, const u16* x, u16* out) {
  bool dead = false;
  lchar* RED = c.L;
  const int l = c.l, g = l >> 4, n = l & 15, w = c.w;
  const unsigned zxo = (2 * o + (n >> 3)) * 16384 + w * 4096 + ((n & 7) + 16 * g) * 16;
  const unsigned zmo = (2 * o + (n >> 3)) * 4096 + w * 1024 + ((n & 7) + 16 * g) * 8;
  const unsigned pxo = (4 * w + (g >> 1)) * 2560 + (g & 1) * 256 + n * 16;
  const unsigned pmo = (4 * w + (g >> 1)) * 2560 + 1024 + (g & 1) * 128 + n * 8;
  const unsigned fz = l < 2 ? FL_Z + (2 * o + l) * 4 : FL_B + (o >> 2) * 4;
  for (int s = 0; s < NSEG; s++) {
    const unsigned fo = OFF_FLAG + s * 2048;
    TS(s, 0);
    wait_flag(c.rs, fz, fo, dead);
    u4 ZX[4][2]; u2 ZM[2][2];
#pragma unroll
    for (int t = 0; t < 4; t++)
#pragma unroll
      for (int hs = 0; hs < 2; hs++) ZX[t][hs] = ld128(c.rs, zxo + t * 1024 + hs * 128, OFF_UX + s * (80 * 16384));
#pragma unroll
    for (int pp = 0; pp < 2; pp++)
#pragma unroll
      for (int hs = 0; hs < 2; hs++) ZM[pp][hs] = ld64(c.rs, zmo + pp * 512 + hs * 64, OFF_UM + s * (80 * 4096));
    u2 xv, bv;
    const int row = 32 * s + 16 * (n >> 3) + 8 * (w & 1) + (n & 7);
    const unsigned eo = row * 640 + o * 16 + g * 4;
    if (w < 2) { xv = *(const u2*)(x + eo); bv = ld64(c.rs, eo * 2, OFF_B); }
    TS(s, 1);
    wait_flag(c.rs, FL_PX + l * 4, fo, dead);
    TS(s, 2);
    const unsigned so = OFF_PX + s * (32 * 2560);
    u4 P[4][2][2]; u2 PM[2][2][2];
#pragma unroll
    for (int t = 0; t < 4; t++)
#pragma unroll
      for (int hs = 0; hs < 2; hs++)
#pragma unroll
        for (int rt = 0; rt < 2; rt++) P[t][hs][rt] = ld128(c.rs, pxo + (2 * (t >> 1) + 16 * hs) * 2560 + (t & 1) * 512 + rt * 1280, so);
#pragma unroll
    for (int pp = 0; pp < 2; pp++)
#pragma unroll
      for (int hs = 0; hs < 2; hs++)
#pragma unroll
        for (int rt = 0; rt < 2; rt++) PM[pp][hs][rt] = ld64(c.rs, pmo + (2 * pp + 16 * hs) * 2560 + rt * 1280, so);
    f4 acc[2][2];
#pragma unroll
    for (int i = 0; i < 2; i++) { acc[i][0] = (f4){0, 0, 0, 0}; acc[i][1] = (f4){0, 0, 0, 0}; }
#pragma unroll
    for (int t = 0; t < 4; t++)
#pragma unroll
      for (int hs = 0; hs < 2; hs++)
#pragma unroll
        for (int rt = 0; rt < 2; rt++) acc[rt][hs] = mfma32(as_bf8(ZX[t][hs]), as_bf8(P[t][hs][rt]), acc[rt][hs]);
#pragma unroll
    for (int pp = 0; pp < 2; pp++)
#pragma unroll
      for (int hs = 0; hs < 2; hs++)
#pragma unroll
        for (int rt = 0; rt < 2; rt++) acc[rt][hs] = mfma16(ZM[pp][hs], PM[pp][hs][rt], acc[rt][hs]);
#pragma unroll
    for (int rt = 0; rt < 2; rt++) lds_w128(RED + (s & 1) * 8192 + (rt * 4 + w) * 1024 + l * 16, __builtin_bit_cast(u4, acc[rt][0] + acc[rt][1]));
    bar();
    if (w < 2) {
      f4 t = {0, 0, 0, 0};
#pragma unroll
      for (int i = 0; i < 4; i++) t += __builtin_bit_cast(f4, lds_r128(RED + (s & 1) * 8192 + (w * 4 + i) * 1024 + l * 16));
      float r0 = bfr(bflo(xv[0]) + bfr(t[0])) + bflo(bv[0]);
      float r1 = bfr(bfhi(xv[0]) + bfr(t[1])) + bfhi(bv[0]);
      float r2 = bfr(bflo(xv[1]) + bfr(t[2])) + bflo(bv[1]);
      float r3 = bfr(bfhi(xv[1]) + bfr(t[3])) + bfhi(bv[1]);
      u2 ov = {pack2(r0, r1), pack2(r2, r3)};
      *(u2*)(out + eo) = ov;
    }
    TS(s, 3);
  }
}

constexpr int IMG_G1 = 36864;
DEV void g1_dma(const Ctx& c, const u16* Wg, const u16* Wu, int cu, int kb, unsigned buf) {
#pragma unroll
  for (int i = 0; i < 9; i++) {
    const int slot = i * 256 + c.tid;
    const int row = slot / 18, cc = slot - row * 18;
    const int k = 64 * kb + swap13(row & 63);
    const u16* src = ((row >> 6) ? Wu : Wg) + (size_t)k * 1536 + cu * 128 + (cc < 16 ? cc : 15) * 8;
    gdma(src, c.lb + buf + (i * 256 + c.w * 64) * 16);
  }
}
DEV void role_G1(const Ctx& c, int cu, const u16* Wg, const u16* Wu) {
  u8v W[4][10];
  const int l = c.l, g = l >> 4, w = c.w;
  {
    const int q = (l >> 2) & 3, p = l & 3;
    const int lb = (16 * (g >> 1) + 4 * (g & 1) + q) * 288 + (32 * w + 4 * p) * 2;
#pragma unroll
    for (int i = 0; i < 4; i++) g1_dma(c, Wg, Wu, cu, i, i * IMG_G1);
#pragma unroll
    for (int kb = 0; kb < 10; kb++) {
      if (kb + 3 < 10) wait_vm<27>(); else wait_vm0();
      bar();
      lchar* bp = c.L + (kb & 3) * IMG_G1 + lb;
#pragma unroll
      for (int tt = 0; tt < 4; tt++) {
        const int o = (tt >> 1) * 64 * 288 + (tt & 1) * 32;
        u2 a = trread(bp + o), b = trread(bp + o + 8 * 288), cc = trread(bp + o + 32 * 288), d = trread(bp + o + 40 * 288);
        u8v f = (u8v){a[0], a[1], b[0], b[1], cc[0], cc[1], d[0], d[1]};
        W[tt][kb] = (kb < 7) ? toA(f) : f;
      }
      bar();
      if (kb + 4 < 10) g1_dma(c, Wg, Wu, cu, kb + 4, (kb & 3) * IMG_G1);
    }
  }
  lin_dma<10, 0>(c, OFF_XNP, 0);
  wait_vm0();
  for (int s = 0; s < NSEG; s++) {
    TS(s, 0);
    wait_vm<2>();
    bar();
    TS(s, 1);
    if (s > 1 && c.tid == 0) st32(1u, c.rs, FL_H + cu * 4, OFF_FLAG + (s - 2) * 2048);
    if (s + 1 < NSEG) lin_dma<10, 0>(c, OFF_XNP + (s + 1) * 40960, ((s + 1) & 1) * 40960);
    lchar* xb = c.L + (s & 1) * 40960 + l * 16;
    f4 acc[2][4];
#pragma unroll
    for (int t = 0; t < 4; t++) { acc[0][t] = (f4){0, 0, 0, 0}; acc[1][t] = (f4){0, 0, 0, 0}; }
    u4 a00 = lds_r128(xb), a01 = lds_r128(xb + 1024), a10 = lds_r128(xb + 20480), a11 = lds_r128(xb + 21504);
#pragma unroll
    for (int kb = 0; kb < 10; kb++) {
      u4 n00, n01, n10, n11;
      if (kb < 9) { lchar* p = xb + (kb + 1) * 2048; n00 = lds_r128(p); n01 = lds_r128(p + 1024); n10 = lds_r128(p + 20480); n11 = lds_r128(p + 21504); }
#pragma unroll
      for (int t = 0; t < 4; t++) { acc[0][t] = mfma32(lo8(W[t][kb]), as_bf8(a00), acc[0][t]); acc[1][t] = mfma32(lo8(W[t][kb]), as_bf8(a10), acc[1][t]); }
#pragma unroll
      for (int t = 0; t < 4; t++) { acc[0][t] = mfma32(hi8(W[t][kb]), as_bf8(a01), acc[0][t]); acc[1][t] = mfma32(hi8(W[t][kb]), as_bf8(a11), acc[1][t]); }
      if (kb < 9) { a00 = n00; a01 = n01; a10 = n10; a11 = n11; }
    }
#pragma unroll
    for (int rt = 0; rt < 2; rt++) {
      float hv[8];
#pragma unroll
      for (int t = 0; t < 2; t++)
#pragma unroll
        for (int i = 0; i < 4; i++) {
          float gg = bfr(acc[rt][t][i]), uu = bfr(acc[rt][2 + t][i]);
          gg = gg * (gg > 0.f ? 1.f : 0.f);
          hv[4 * t + i] = gg * uu;
        }
      u4 o = {pack2(hv[0], hv[1]), pack2(hv[2], hv[3]), pack2(hv[4], hv[5]), pack2(hv[6], hv[7])};
      st128(o, c.rs, (4 * cu + w) * 1024 + l * 16, OFF_H + (s * 2 + rt) * 49152);
    }
  }
  wait_vm0();
  bar();
  if (c.tid < 2) st32(1u, c.rs, FL_H + cu * 4 + c.tid * 2048, OFF_FLAG + (NSEG - 2) * 2048);
}

constexpr int IMG_G2 = 40960;
DEV void g2_dma(const Ctx& c, const u16* Wd, int cu, int ch, unsigned buf) {
#pragma unroll
  for (int i = 0; i < 10; i++) {
    const int slot = i * 256 + c.tid;
    const int row = slot / 10, cc = slot - row * 10;
    const u16* src = Wd + (size_t)(256 * ch + row) * 640 + cu * 64 + (cc < 8 ? cc : 7) * 8;
    gdma(src, c.lb + buf + (i * 256 + c.w * 64) * 16);
  }
}
DEV void role_G2(const Ctx& c, int cu, const u16* Wd) {
  bool dead = false;
  u4 W[48];
  const int l = c.l, g = l >> 4, n = l & 15, w = c.w;
  {
    const int q = (l >> 2) & 3, p = l & 3;
    const int lb = (4 * g + q) * 160 + (16 * w + 4 * p) * 2;
#pragma unroll
    for (int i = 0; i < 3; i++) g2_dma(c, Wd, cu, i, i * IMG_G2);
#pragma unroll
    for (int ch = 0; ch < 6; ch++) {
      if (ch + 2 < 6) wait_vm<20>(); else wait_vm0();
      bar();
      lchar* bp = c.L + (ch % 3) * IMG_G2 + lb;
#pragma unroll
      for (int u = 0; u < 8; u++) {
        u2 a = trread(bp + u * 32 * 160), b = trread(bp + (u * 32 + 16) * 160);
        W[ch * 8 + u] = toA4((u4){a[0], a[1], b[0], b[1]});
      }
      bar();
      if (ch + 3 < 6) g2_dma(c, Wd, cu, ch + 3, (ch % 3) * IMG_G2);
    }
  }
  const unsigned hfo = FL_H + (l < 12 ? l : 11) * 4;
  wait_flag(c.rs, hfo, OFF_FLAG, dead);
  lin_dma<12, 1>(c, OFF_H, 0);
  wait_vm0();
  for (int s = 0; s < NSEG; s++) {
#pragma unroll
    for (int rt = 0; rt < 2; rt++) {
      TS(s, rt * 2);
      wait_vm<1>();
      bar();
      TS(s, rt * 2 + 1);
      if (rt == 1 && s > 0 && c.tid == 0) st32(1u, c.rs, FL_B + cu * 4, OFF_FLAG + (s - 1) * 2048);
      const int nx = 2 * s + rt + 1;
      if (nx < 2 * NSEG) {
        if (rt == 1) wait_flag(c.rs, hfo, OFF_FLAG + (s + 1) * 2048, dead);
        lin_dma<12, 1>(c, OFF_H + nx * 49152, (rt ^ 1) * 49152);
      }
      lchar* hb = c.L + rt * 49152 + l * 16;
      f4 acc[4];
#pragma unroll
      for (int i = 0; i < 4; i++) acc[i] = (f4){0, 0, 0, 0};
      u4 hf[8];
#pragma unroll
      for (int u = 0; u < 8; u++) hf[u] = lds_r128(hb + u * 1024);
#pragma unroll
      for (int u = 0; u < 48; u++) {
        acc[u & 3] = mfma32(as_bf8(W[u]), as_bf8(hf[u & 7]), acc[u & 3]);
        if (u + 8 < 48) hf[u & 7] = lds_r128(hb + (u + 8) * 1024);
      }
      f4 t = (acc[0] + acc[1]) + (acc[2] + acc[3]);
      const int row = 32 * s + 16 * (n >> 3) + 8 * rt + (n & 7);
      u2 o = {pack2(t[0], t[1]), pack2(t[2], t[3])};
      st64(o, c.rs, (row * 640 + cu * 64 + w * 16 + 4 * g) * 2, OFF_B);
    }
  }
  wait_vm0();
  bar();
  if (c.tid == 0) st32(1u, c.rs, FL_B + cu * 4, OFF_FLAG + (NSEG - 1) * 2048);
}

__global__ void __launch_bounds__(256, 1) k_main(const u16* __restrict__ x, const u16* __restrict__ Wq, const u16* __restrict__ Wk,
                                                 const u16* __restrict__ Wg, const u16* __restrict__ Wu, const u16* __restrict__ Wd,
                                                 u16* __restrict__ out, char* __restrict__ ws) {
  __shared__ __attribute__((aligned(16))) char smem[163840];
  Ctx c;
  c.L = (lchar*)smem;
  c.lb = (unsigned)(uintptr_t)c.L;
  c.ws = ws;
  c.rs = __builtin_amdgcn_make_buffer_rsrc(ws, 0, WS_USED, 0x00020000);
  c.srd = (u4){(unsigned)(uintptr_t)ws, (unsigned)((uintptr_t)ws >> 32) & 0xFFFFu, WS_USED, 0x00020000u};
  c.tid = threadIdx.x;
  c.l = threadIdx.x & 63;
  c.w = __builtin_amdgcn_readfirstlane(threadIdx.x >> 6);
  const int b = blockIdx.x;
  TS(67, 0);
  if (b < 32) role_S(c, b, Wq, Wk);
  else if (b < 112) role_R(c, b - 32);
  else if (b < 144) role_X(c, b - 112, Wq, Wk);
  else if (b < 160) role_A(c, b - 144);
  else if (b < 200) role_O(c, b - 160, x, out);
  else if (b < 212) role_G1(c, b - 200, Wg, Wu);
  else if (b < 222) role_G2(c, b - 212, Wd);
  TS(67, 1);
}

extern "C" void kernel_launch(void* const* d_in, const int* in_sizes, int n_in, void* d_out, int out_size, void* d_ws, size_t ws_size,
                              hipStream_t stream) {
  const u16* x = (const u16*)d_in[0];
  const u16* mem0 = (const u16*)d_in[1];
  const u16* Wq = (const u16*)d_in[2];
  const u16* Wk = (const u16*)d_in[3];
  const u16* Wv = (const u16*)d_in[4];
  const u16* Wo = (const u16*)d_in[5];
  const u16* Wg = (const u16*)d_in[8];
  const u16* Wu = (const u16*)d_in[9];
  const u16* Wd = (const u16*)d_in[10];
  char* ws = (char*)d_ws;
  k_prep<<<640, 256, 0, stream>>>(x, mem0, ws);
  k_dgemm<<<800, 256, 0, stream>>>(Wv, Wo, ws);
  k_main<<<222, 256, 0, stream>>>(x, Wq, Wk, Wg, Wu, Wd, (u16*)d_out, ws);
}
```

```cpp
#include <hip/hip_runtime.h>
#include <stdint.h>

typedef __attribute__((ext_vector_type(2))) __bf16 bf2;
typedef __attribute__((ext_vector_type(4))) __bf16 bf4;
typedef __attribute__((ext_vector_type(8))) __bf16 bf8;
typedef __attribute__((ext_vector_type(16))) __bf16 bf16v;
typedef __attribute__((ext_vector_type(4))) float f4;
typedef __attribute__((ext_vector_type(2))) unsigned u2;
typedef __attribute__((ext_vector_type(4))) unsigned u4;
typedef __attribute__((ext_vector_type(8))) unsigned u8v;
typedef __attribute__((ext_vector_type(4))) short s4v;
typedef __attribute__((address_space(3))) char lchar;
typedef __attribute__((address_space(1))) const void gvoid;
typedef unsigned short u16;

#define DEV __device__ __forceinline__
#define CB() asm volatile("" ::: "memory")
#define SENT 0xFFFFFFFFu

constexpr int NSEG = 64;
constexpr unsigned MB = 1u << 20;
constexpr unsigned OFF_XNP = 0;
#define DWP 0
#define DJP 0
#define DAUX 16
constexpr unsigned OFF_D = 5 * MB / 2, DWVS = 81920 + DWP, DJS = 4 * DWVS + DJP;
constexpr unsigned OFF_COMM = 28 * MB;
constexpr unsigned OFF_FLAG = 35 * MB;
constexpr unsigned OFF_KX = 40 * MB;
constexpr unsigned OFF_QX = 64 * MB;
constexpr unsigned OFF_PX = 88 * MB;
constexpr unsigned OFF_H = 94 * MB;
constexpr unsigned OFF_B = 100 * MB;
constexpr unsigned OFF_UX = 104 * MB;
constexpr unsigned UXJ = 65 * 20480 + 256;
constexpr unsigned WS_USED = 220 * MB;
constexpr unsigned OFF_TICK = OFF_FLAG + 64 * 2048, OFF_VOTE = OFF_TICK + 2048;
constexpr unsigned FL_KX = 0, FL_QX = 128, FL_H = 512, FL_B = 640;
constexpr unsigned OFF_PXF = OFF_FLAG + 0x40000, OFF_ZF = OFF_PXF + 0x40000;
constexpr int SPIN_MAX = 1 << 18;

DEV unsigned pack2(float a, float b) { bf2 v = {(__bf16)a, (__bf16)b}; return __builtin_bit_cast(unsigned, v); }
DEV float bfr(float a) { return (float)(__bf16)a; }
DEV float bflo(unsigned d) { return __builtin_bit_cast(float, d << 16); }
DEV float bfhi(unsigned d) { return __builtin_bit_cast(float, d & 0xFFFF0000u); }
DEV unsigned nosent(unsigned d) { return d < 0xFFFFFFFEu ? d : 0xFFFFFFFEu; }
DEV bf8 as_bf8(u4 v) { return __builtin_bit_cast(bf8, v); }
DEV bf8 lo8(const u8v& v) { u4 t = {v[0], v[1], v[2], v[3]}; return __builtin_bit_cast(bf8, t); }
DEV bf8 hi8(const u8v& v) { u4 t = {v[4], v[5], v[6], v[7]}; return __builtin_bit_cast(bf8, t); }
DEV f4 mfma32(bf8 a, bf8 b, f4 c) { return __builtin_amdgcn_mfma_f32_16x16x32_bf16(a, b, c, 0, 0, 0); }
DEV f4 mfma16(u2 a, u2 b, f4 c) {
  return __builtin_amdgcn_mfma_f32_16x16x16bf16_1k(__builtin_bit_cast(s4v, a), __builtin_bit_cast(s4v, b), c, 0, 0, 0);
}
DEV f4 smf(u4 a, const u8v& b, f4 c, int idx) {
  return __builtin_amdgcn_smfmac_f32_16x16x64_bf16(__builtin_bit_cast(bf8, a), __builtin_bit_cast(bf16v, b), c, idx, 0, 0);
}
DEV u2 trread(lchar* p) {
  bf4 v = __builtin_amdgcn_ds_read_tr16_b64_v4bf16((__attribute__((address_space(3))) bf4*)p);
  return __builtin_bit_cast(u2, v);
}
DEV u4 lds_r128(lchar* p) { return *(__attribute__((address_space(3))) u4*)p; }
DEV void lds_w128(lchar* p, u4 v) { *(__attribute__((address_space(3))) u4*)p = v; }
DEV void lds_w64(lchar* p, u2 v) { *(__attribute__((address_space(3))) u2*)p = v; }
DEV void gdma(const void* g, unsigned m0v) {
  asm volatile("s_mov_b32 m0, %1\n\ts_nop 0\n\tglobal_load_lds_dwordx4 %0, off" :: "v"(g), "s"(m0v) : "memory");
}
template <int IMM, int SC1> DEV void bdma(u4 srd, unsigned voff, unsigned soff, unsigned m0v) {
  if (SC1) asm volatile("s_mov_b32 m0, %3\n\ts_nop 0\n\tbuffer_load_dwordx4 %0, %1, %2 offen offset:%4 sc1 lds" :: "v"(voff), "s"(srd), "s"(soff), "s"(m0v), "i"(IMM) : "memory");
  else asm volatile("s_mov_b32 m0, %3\n\ts_nop 0\n\tbuffer_load_dwordx4 %0, %1, %2 offen offset:%4 lds" :: "v"(voff), "s"(srd), "s"(soff), "s"(m0v), "i"(IMM) : "memory");
}
DEV u8v toA(u8v v) { u8v r; asm("" : "=a"(r) : "0"(v)); return r; }
DEV u4 toA4(u4 v) { u4 r; asm("" : "=a"(r) : "0"(v)); return r; }
template <int N> DEV void wait_vm() { asm volatile("s_waitcnt vmcnt(%0)" :: "i"(N) : "memory"); }
typedef __amdgpu_buffer_rsrc_t rsrc_t;
DEV u4 ld128(rsrc_t r, unsigned v, unsigned s) { CB(); u4 x = __builtin_amdgcn_raw_buffer_load_b128(r, v, s, 16); CB(); return x; }
DEV u2 ld64(rsrc_t r, unsigned v, unsigned s) { CB(); u2 x = __builtin_amdgcn_raw_buffer_load_b64(r, v, s, 16); CB(); return x; }
DEV unsigned ld32(rsrc_t r, unsigned v, unsigned s) { CB(); unsigned x = __builtin_amdgcn_raw_buffer_load_b32(r, v, s, 16); CB(); return x; }
DEV void st128(u4 d, rsrc_t r, unsigned v, unsigned s) { __builtin_amdgcn_raw_buffer_store_b128(d, r, v, s, 16); }
DEV void st64(u2 d, rsrc_t r, unsigned v, unsigned s) { __builtin_amdgcn_raw_buffer_store_b64(d, r, v, s, 16); }
DEV void st32(unsigned d, rsrc_t r, unsigned v, unsigned s) { __builtin_amdgcn_raw_buffer_store_b32(d, r, v, s, 16); }
DEV void wait_vm0() { asm volatile("s_waitcnt vmcnt(0)" ::: "memory"); }
DEV void wait_lgkm0() { asm volatile("s_waitcnt lgkmcnt(0)" ::: "memory"); }
DEV void bar() { asm volatile("s_waitcnt lgkmcnt(0)\n\ts_barrier" ::: "memory"); }
DEV unsigned umax4(u4 v) { return max(max(v[0], v[1]), max(v[2], v[3])); }
DEV float u2f(unsigned u) { return __builtin_bit_cast(float, u); }
DEV unsigned f2u(float f) { return __builtin_bit_cast(unsigned, f); }
DEV float xmax32(float v) { u2 r = __builtin_amdgcn_permlane32_swap(f2u(v), f2u(v), false, false); unsigned a = r[0], b = r[1]; return fmaxf(u2f(a), u2f(b)); }
DEV float xmax16(float v) { u2 r = __builtin_amdgcn_permlane16_swap(f2u(v), f2u(v), false, false); unsigned a = r[0], b = r[1]; return fmaxf(u2f(a), u2f(b)); }
DEV float xsum32(float v) { u2 r = __builtin_amdgcn_permlane32_swap(f2u(v), f2u(v), false, false); unsigned a = r[0], b = r[1]; return u2f(a) + u2f(b); }
DEV float xsum16(float v) { u2 r = __builtin_amdgcn_permlane16_swap(f2u(v), f2u(v), false, false); unsigned a = r[0], b = r[1]; return u2f(a) + u2f(b); }
DEV float halfsum(float a, float b) {
  u2 r = __builtin_amdgcn_permlane32_swap(f2u(a), f2u(b), false, false);
  unsigned x = r[0], y = r[1];
  return u2f(x) + u2f(y);
}

DEV void wait_flag(rsrc_t r, unsigned voff, unsigned soff, bool& dead) {
  if (dead) return;
  for (int i = 0; i < SPIN_MAX; ++i) {
    unsigned f = ld32(r, voff, soff);
    if (__all(f != 0)) return;
    __builtin_amdgcn_s_sleep(1);
  }
  dead = true;
}

DEV unsigned comm_base(const char* ws) { return OFF_COMM + ((0u - (unsigned)(uintptr_t)ws) & (2 * MB - 1)); }

#define S_ASM \
"s_nop 4\n" \
"s_mov_b32 s18, 0\n" \
"s_mov_b32 s19, 0\n" \
"s_mov_b32 s21, s15\n" \
"s_lshr_b32 s24, s12, 3\n" \
"s_lshl_b32 s24, s24, 14\n" \
"s_and_b32 s25, s12, 7\n" \
"s_mul_i32 s25, s25, 640\n" \
"s_add_u32 s24, s24, s25\n" \
"s_add_u32 s24, s24, s15\n" \
"s_lshl_b32 s27, s12, 2\n" \
"s_add_u32 s27, s27, 36700160\n" \
"s_lshl_b32 s37, s12, 7\n" \
"s_add_u32 s37, s37, 36958208\n" \
"s_mul_i32 s28, s12, 786432\n" \
"s_add_u32 s26, s28, 67108864\n" \
"s_add_u32 s28, s28, 41943040\n" \
"s_mul_i32 s25, s12, 2560\n" \
"s_mul_i32 s29, s13, 1280\n" \
"s_add_u32 s25, s25, s29\n" \
"s_add_u32 s25, s25, 92273408\n" \
"s_mov_b32 s29, 16384\n" \
"s_mov_b32 s30, 0x3dd53b95\n" \
"s_mov_b32 s32, 0x00ff00ff\n" \
"s_mov_b32 s33, 0x00ff00ff\n" \
"s_mov_b32 s22, 0x00ff00ff\n" \
"s_mov_b32 s23, 0\n" \
"s_mov_b32 s16, 0\n" \
"s_mov_b32 s17, -1\n" \
"v_cmp_le_i32 s[64:65], 0, v127\n" \
"v_cmp_le_i32 s[66:67], 1, v127\n" \
"v_cmp_le_i32 s[68:69], 2, v127\n" \
"v_cmp_le_i32 s[70:71], 3, v127\n" \
"v_cmp_le_i32 s[72:73], 8, v127\n" \
"v_cmp_le_i32 s[74:75], 9, v127\n" \
"v_cmp_le_i32 s[76:77], 10, v127\n" \
"v_cmp_le_i32 s[78:79], 11, v127\n" \
"v_add_u32 v127, 16384, v114\n" \
"s_add_u32 s31, s3, 96\n" \
"s_mov_b32 s58, 85\n" \
"s_cmp_eq_u32 s13, 1\n" \
"s_cbranch_scc1 LS1\n" \
"s_cmp_eq_u32 s13, 2\n" \
"s_cbranch_scc1 LS2\n" \
"s_cmp_eq_u32 s13, 3\n" \
"s_cbranch_scc1 LS3\n" \
"LS0:\n" \
"s_branch LS0_go\n" \
".p2align 6\n" \
"LS0_loop:\n" \
"s_waitcnt lgkmcnt(0)\n" \
"LS0_tm:\n" \
"s_memrealtime s[38:39]\n" \
"s_waitcnt lgkmcnt(0)\n" \
"s_sub_u32 s38, s38, s62\n" \
"s_cmp_lt_i32 s38, 0\n" \
"s_cbranch_scc1 LS0_tm\n" \
"LS0_go:\n" \
"buffer_load_dwordx4 v[0:3], v112, s[8:11], s21 offen sc1\n" \
"buffer_load_dwordx4 v[4:7], v112, s[8:11], s21 offen offset:1024 sc1\n" \
"buffer_load_dwordx2 v[8:9], v113, s[8:11], s21 offen sc1\n" \
"ds_read_b128 v[64:67], v115 offset:0\n" \
"ds_read_b128 v[68:71], v115 offset:1024\n" \
"ds_read_b128 v[72:75], v115 offset:2048\n" \
"ds_read_b128 v[76:79], v115 offset:3072\n" \
"ds_read_b128 v[80:83], v115 offset:4096\n" \
"ds_read_b128 v[84:87], v115 offset:5120\n" \
"ds_read_b128 v[88:91], v115 offset:12288\n" \
"ds_read_b128 v[92:95], v115 offset:13312\n" \
"ds_read_b128 v[96:99], v115 offset:14336\n" \
"ds_read_b128 v[100:103], v115 offset:15360\n" \
"ds_read_b128 v[104:107], v115 offset:16384\n" \
"ds_read_b128 v[108:111], v115 offset:17408\n" \
"v_mov_b64 v[40:41], 0\n" \
"v_mov_b64 v[42:43], 0\n" \
"v_mov_b64 v[44:45], 0\n" \
"v_mov_b64 v[46:47], 0\n" \
"v_mov_b64 v[48:49], 0\n" \
"v_mov_b64 v[50:51], 0\n" \
"v_mov_b64 v[52:53], 0\n" \
"v_mov_b64 v[54:55], 0\n" \
"v_mov_b64 v[56:57], 0\n" \
"v_mov_b64 v[58:59], 0\n" \
"v_mov_b64 v[60:61], 0\n" \
"v_mov_b64 v[62:63], 0\n" \
"s_waitcnt lgkmcnt(0)\n" \
"s_mov_b32 s20, 262144\n" \
"LS0_q:\n" \
"s_waitcnt vmcnt(2)\n" \
"ds_write_b128 v124, v[0:3] offset:0\n" \
"v_smfmac_f32_16x16x64_bf16 v[40:43], v[0:3], a[0:7], v116\n" \
"s_waitcnt vmcnt(1)\n" \
"ds_write_b128 v124, v[4:7] offset:1024\n" \
"v_max3_u32 v122, v0, v1, v2\n" \
"v_max3_u32 v122, v122, v3, v4\n" \
"v_smfmac_f32_16x16x64_bf16 v[44:47], v[0:3], a[8:15], v116\n" \
"s_waitcnt vmcnt(0)\n" \
"ds_write_b64 v125, v[8:9] offset:0\n" \
"v_max3_u32 v122, v122, v5, v6\n" \
"v_max3_u32 v122, v122, v7, v8\n" \
"v_smfmac_f32_16x16x64_bf16 v[48:51], v[0:3], a[16:23], v116\n" \
"v_max_u32 v122, v122, v9\n" \
"v_cmp_eq_u32 vcc, -1, v122\n" \
"s_cbranch_vccnz LS0_qr\n" \
"LS0_q_soft:\n" \
"s_waitcnt lgkmcnt(0)\n" \
"s_barrier\n" \
"v_smfmac_f32_16x16x64_bf16 v[40:43], v[4:7], a[48:55], v116\n" \
"ds_read_b128 v[8:11], v114 offset:2048\n" \
"v_smfmac_f32_16x16x64_bf16 v[44:47], v[4:7], a[56:63], v116\n" \
"ds_read_b128 v[28:31], v114 offset:7168\n" \
"v_smfmac_f32_16x16x64_bf16 v[48:51], v[4:7], a[64:71], v116\n" \
"ds_read_b128 v[12:15], v114 offset:3072\n" \
"v_smfmac_f32_16x16x64_bf16 v[52:55], v[0:3], a[24:31], v116\n" \
"ds_read_b128 v[16:19], v114 offset:4096\n" \
"v_smfmac_f32_16x16x64_bf16 v[56:59], v[0:3], a[32:39], v116\n" \
"ds_read_b128 v[20:23], v114 offset:5120\n" \
"v_smfmac_f32_16x16x64_bf16 v[60:63], v[0:3], a[40:47], v116\n" \
"ds_read_b128 v[24:27], v114 offset:6144\n" \
"v_smfmac_f32_16x16x64_bf16 v[52:55], v[4:7], a[72:79], v116\n" \
"ds_read_b128 v[32:35], v114 offset:8192\n" \
"v_smfmac_f32_16x16x64_bf16 v[56:59], v[4:7], a[80:87], v116\n" \
"ds_read_b128 v[36:39], v114 offset:9216\n" \
"v_smfmac_f32_16x16x64_bf16 v[60:63], v[4:7], a[88:95], v116\n" \
"s_waitcnt lgkmcnt(7)\n" \
"v_smfmac_f32_16x16x64_bf16 v[40:43], v[8:11], v[64:71], v116\n" \
"v_smfmac_f32_16x16x64_bf16 v[44:47], v[8:11], v[72:79], v116\n" \
"v_smfmac_f32_16x16x64_bf16 v[48:51], v[8:11], v[80:87], v116\n" \
"s_waitcnt lgkmcnt(6)\n" \
"v_smfmac_f32_16x16x64_bf16 v[40:43], v[28:31], v[88:95], v116\n" \
"v_smfmac_f32_16x16x64_bf16 v[44:47], v[28:31], v[96:103], v116\n" \
"v_smfmac_f32_16x16x64_bf16 v[48:51], v[28:31], v[104:111], v116\n" \
"s_waitcnt lgkmcnt(5)\n" \
"v_smfmac_f32_16x16x64_bf16 v[40:43], v[12:15], a[96:103], v116\n" \
"s_waitcnt lgkmcnt(4)\n" \
"v_smfmac_f32_16x16x64_bf16 v[40:43], v[16:19], a[144:151], v116\n" \
"s_waitcnt lgkmcnt(3)\n" \
"v_smfmac_f32_16x16x64_bf16 v[40:43], v[20:23], a[192:199], v116\n" \
"ds_read_b128 v[64:67], v115 offset:6144\n" \
"s_waitcnt lgkmcnt(3)\n" \
"v_smfmac_f32_16x16x64_bf16 v[40:43], v[24:27], a[240:247], v116\n" \
"ds_read_b128 v[68:71], v115 offset:7168\n" \
"s_waitcnt lgkmcnt(3)\n" \
"v_smfmac_f32_16x16x64_bf16 v[40:43], v[32:35], v[160:167], v116\n" \
"ds_read_b128 v[72:75], v115 offset:8192\n" \
"s_waitcnt lgkmcnt(3)\n" \
"v_smfmac_f32_16x16x64_bf16 v[40:43], v[36:39], v[208:215], v116\n" \
"ds_read_b128 v[76:79], v115 offset:9216\n" \
"v_smfmac_f32_16x16x64_bf16 v[44:47], v[12:15], a[104:111], v116\n" \
"ds_read_b128 v[80:83], v115 offset:10240\n" \
"v_smfmac_f32_16x16x64_bf16 v[44:47], v[16:19], a[152:159], v116\n" \
"ds_read_b128 v[84:87], v115 offset:11264\n" \
"v_smfmac_f32_16x16x64_bf16 v[44:47], v[20:23], a[200:207], v116\n" \
"ds_read_b128 v[88:91], v115 offset:18432\n" \
"v_permlane32_swap_b32 v40, v42\n" \
"v_smfmac_f32_16x16x64_bf16 v[44:47], v[24:27], a[248:255], v116\n" \
"ds_read_b128 v[92:95], v115 offset:19456\n" \
"v_permlane32_swap_b32 v41, v43\n" \
"v_smfmac_f32_16x16x64_bf16 v[44:47], v[32:35], v[168:175], v116\n" \
"ds_read_b128 v[96:99], v115 offset:20480\n" \
"v_add_f32 v40, v40, v42\n" \
"v_smfmac_f32_16x16x64_bf16 v[44:47], v[36:39], v[216:223], v116\n" \
"ds_read_b128 v[100:103], v115 offset:21504\n" \
"v_add_f32 v41, v41, v43\n" \
"v_smfmac_f32_16x16x64_bf16 v[48:51], v[12:15], a[112:119], v116\n" \
"ds_read_b128 v[104:107], v115 offset:22528\n" \
"v_cvt_pk_bf16_f32 v122, v40, v41\n" \
"v_smfmac_f32_16x16x64_bf16 v[48:51], v[16:19], a[160:167], v116\n" \
"ds_write_b32 v117, v122 offset:0\n" \
"v_smfmac_f32_16x16x64_bf16 v[48:51], v[20:23], a[208:215], v116\n" \
"ds_read_b128 v[108:111], v115 offset:23552\n" \
"v_permlane32_swap_b32 v44, v46\n" \
"v_smfmac_f32_16x16x64_bf16 v[48:51], v[24:27], v[128:135], v116\n" \
"v_permlane32_swap_b32 v45, v47\n" \
"v_smfmac_f32_16x16x64_bf16 v[48:51], v[32:35], v[176:183], v116\n" \
"v_add_f32 v44, v44, v46\n" \
"v_smfmac_f32_16x16x64_bf16 v[48:51], v[36:39], v[224:231], v116\n" \
"v_add_f32 v45, v45, v47\n" \
"s_waitcnt lgkmcnt(11)\n" \
"v_smfmac_f32_16x16x64_bf16 v[52:55], v[8:11], v[64:71], v116\n" \
"v_cvt_pk_bf16_f32 v123, v44, v45\n" \
"s_waitcnt lgkmcnt(9)\n" \
"v_smfmac_f32_16x16x64_bf16 v[56:59], v[8:11], v[72:79], v116\n" \
"ds_write_b32 v117, v123 offset:512\n" \
"s_waitcnt lgkmcnt(8)\n" \
"v_smfmac_f32_16x16x64_bf16 v[60:63], v[8:11], v[80:87], v116\n" \
"v_permlane32_swap_b32 v48, v50\n" \
"s_waitcnt lgkmcnt(6)\n" \
"v_smfmac_f32_16x16x64_bf16 v[52:55], v[28:31], v[88:95], v116\n" \
"v_permlane32_swap_b32 v49, v51\n" \
"s_waitcnt lgkmcnt(4)\n" \
"v_smfmac_f32_16x16x64_bf16 v[56:59], v[28:31], v[96:103], v116\n" \
"v_add_f32 v48, v48, v50\n" \
"s_waitcnt lgkmcnt(1)\n" \
"v_smfmac_f32_16x16x64_bf16 v[60:63], v[28:31], v[104:111], v116\n" \
"v_add_f32 v49, v49, v51\n" \
"v_smfmac_f32_16x16x64_bf16 v[52:55], v[12:15], a[120:127], v116\n" \
"v_cvt_pk_bf16_f32 v122, v48, v49\n" \
"v_smfmac_f32_16x16x64_bf16 v[52:55], v[16:19], a[168:175], v116\n" \
"ds_write_b32 v117, v122 offset:1024\n" \
"v_smfmac_f32_16x16x64_bf16 v[52:55], v[20:23], a[216:223], v116\n" \
"s_waitcnt lgkmcnt(0)\n" \
"s_barrier\n" \
"v_smfmac_f32_16x16x64_bf16 v[52:55], v[24:27], v[136:143], v116\n" \
"ds_read_b64_tr_b16 v[64:65], v118 offset:0\n" \
"v_smfmac_f32_16x16x64_bf16 v[52:55], v[32:35], v[184:191], v116\n" \
"ds_read_b64_tr_b16 v[66:67], v118 offset:512\n" \
"v_smfmac_f32_16x16x64_bf16 v[52:55], v[36:39], v[232:239], v116\n" \
"ds_read_b64_tr_b16 v[68:69], v118 offset:1024\n" \
"v_smfmac_f32_16x16x64_bf16 v[56:59], v[12:15], a[128:135], v116\n" \
"ds_read_b64_tr_b16 v[70:71], v118 offset:1536\n" \
"v_smfmac_f32_16x16x64_bf16 v[56:59], v[16:19], a[176:183], v116\n" \
"ds_read_b64_tr_b16 v[72:73], v118 offset:2048\n" \
"v_smfmac_f32_16x16x64_bf16 v[56:59], v[20:23], a[224:231], v116\n" \
"ds_read_b64_tr_b16 v[74:75], v118 offset:2560\n" \
"v_permlane32_swap_b32 v52, v54\n" \
"v_smfmac_f32_16x16x64_bf16 v[56:59], v[24:27], v[144:151], v116\n" \
"ds_read_b64_tr_b16 v[76:77], v118 offset:3072\n" \
"v_permlane32_swap_b32 v53, v55\n" \
"v_smfmac_f32_16x16x64_bf16 v[56:59], v[32:35], v[192:199], v116\n" \
"ds_read_b64_tr_b16 v[78:79], v118 offset:3584\n" \
"v_add_f32 v52, v52, v54\n" \
"v_smfmac_f32_16x16x64_bf16 v[56:59], v[36:39], v[240:247], v116\n" \
"ds_read_b64_tr_b16 v[80:81], v118 offset:4096\n" \
"v_add_f32 v53, v53, v55\n" \
"v_smfmac_f32_16x16x64_bf16 v[60:63], v[12:15], a[136:143], v116\n" \
"ds_read_b64_tr_b16 v[82:83], v118 offset:4608\n" \
"v_cvt_pk_bf16_f32 v123, v52, v53\n" \
"v_smfmac_f32_16x16x64_bf16 v[60:63], v[16:19], a[184:191], v116\n" \
"ds_read_b64_tr_b16 v[84:85], v118 offset:5120\n" \
"v_smfmac_f32_16x16x64_bf16 v[60:63], v[20:23], a[232:239], v116\n" \
"ds_read_b64_tr_b16 v[86:87], v118 offset:5632\n" \
"v_permlane32_swap_b32 v56, v58\n" \
"v_smfmac_f32_16x16x64_bf16 v[60:63], v[24:27], v[152:159], v116\n" \
"ds_write_b32 v117, v123 offset:16\n" \
"v_permlane32_swap_b32 v57, v59\n" \
"v_smfmac_f32_16x16x64_bf16 v[60:63], v[32:35], v[200:207], v116\n" \
"v_add_f32 v56, v56, v58\n" \
"v_smfmac_f32_16x16x64_bf16 v[60:63], v[36:39], v[248:255], v116\n" \
"v_add_f32 v57, v57, v59\n" \
"v_cvt_pk_bf16_f32 v122, v56, v57\n" \
"ds_write_b32 v117, v122 offset:528\n" \
"s_nop 4\n" \
"v_permlane32_swap_b32 v60, v62\n" \
"v_permlane32_swap_b32 v61, v63\n" \
"v_add_f32 v60, v60, v62\n" \
"v_add_f32 v61, v61, v63\n" \
"v_cvt_pk_bf16_f32 v123, v60, v61\n" \
"ds_write_b32 v117, v123 offset:1040\n" \
"s_waitcnt lgkmcnt(0)\n" \
"s_barrier\n" \
"ds_read_b64_tr_b16 v[88:89], v119 offset:0\n" \
"ds_read_b64_tr_b16 v[90:91], v119 offset:512\n" \
"ds_read_b64_tr_b16 v[92:93], v119 offset:1024\n" \
"ds_read_b64_tr_b16 v[94:95], v119 offset:1536\n" \
"ds_read_b64_tr_b16 v[96:97], v119 offset:2048\n" \
"ds_read_b64_tr_b16 v[98:99], v119 offset:2560\n" \
"ds_read_b64_tr_b16 v[100:101], v119 offset:3072\n" \
"ds_read_b64_tr_b16 v[102:103], v119 offset:3584\n" \
"ds_read_b64_tr_b16 v[104:105], v119 offset:4096\n" \
"ds_read_b64_tr_b16 v[106:107], v119 offset:4608\n" \
"ds_read_b64_tr_b16 v[108:109], v119 offset:5120\n" \
"ds_read_b64_tr_b16 v[110:111], v119 offset:5632\n" \
"s_waitcnt lgkmcnt(10)\n" \
"v_mfma_f32_16x16x32_bf16 v[40:43], v[88:91], v[64:67], 0\n" \
"s_waitcnt lgkmcnt(8)\n" \
"v_mfma_f32_16x16x32_bf16 v[40:43], v[92:95], v[68:71], v[40:43]\n" \
"s_barrier\n" \
"ds_read_b64 v[8:9], v126 offset:43008\n" \
"ds_read_b64 v[10:11], v126 offset:43520\n" \
"ds_read_b128 v[0:3], v114 offset:40960\n" \
"ds_read_b128 v[4:7], v114 offset:41984\n" \
"s_waitcnt lgkmcnt(10)\n" \
"v_mfma_f32_16x16x32_bf16 v[40:43], v[96:99], v[72:75], v[40:43]\n" \
"s_waitcnt lgkmcnt(8)\n" \
"v_mfma_f32_16x16x32_bf16 v[40:43], v[100:103], v[76:79], v[40:43]\n" \
"s_waitcnt lgkmcnt(6)\n" \
"v_mfma_f32_16x16x32_bf16 v[40:43], v[104:107], v[80:83], v[40:43]\n" \
"s_waitcnt lgkmcnt(4)\n" \
"v_mfma_f32_16x16x32_bf16 v[40:43], v[108:111], v[84:87], v[40:43]\n" \
"s_waitcnt lgkmcnt(2)\n" \
"v_max_f32 v12, v8, v10\n" \
"v_mul_f32 v13, s30, v12\n" \
"v_fma_f32 v14, v8, s30, -v13\n" \
"v_fma_f32 v15, v10, s30, -v13\n" \
"v_exp_f32 v14, v14\n" \
"v_exp_f32 v15, v15\n" \
"s_nop 0\n" \
"v_max3_f32 v18, v40, v41, v42\n" \
"v_max3_f32 v18, v18, v43, v12\n" \
"v_mov_b32 v19, v18\n" \
"v_mul_f32 v16, v9, v14\n" \
"v_fma_f32 v16, v11, v15, v16\n" \
"v_permlane16_swap_b32 v18, v19\n" \
"v_max_f32 v18, v18, v19\n" \
"v_mov_b32 v17, v16\n" \
"v_mul_f32 v18, s30, v18\n" \
"v_sub_f32 v20, v13, v18\n" \
"v_permlane16_swap_b32 v16, v17\n" \
"v_fma_f32 v40, v40, s30, -v18\n" \
"v_fma_f32 v41, v41, s30, -v18\n" \
"v_fma_f32 v42, v42, s30, -v18\n" \
"v_fma_f32 v43, v43, s30, -v18\n" \
"v_exp_f32 v20, v20\n" \
"v_add_f32 v16, v16, v17\n" \
"v_exp_f32 v40, v40\n" \
"v_exp_f32 v41, v41\n" \
"v_exp_f32 v42, v42\n" \
"v_exp_f32 v43, v43\n" \
"v_mov_b32 v17, v16\n" \
"v_mul_f32 v14, v14, v20\n" \
"v_add_f32 v21, v40, v41\n" \
"v_permlane32_swap_b32 v16, v17\n" \
"v_add_f32 v22, v42, v43\n" \
"v_add_f32 v21, v21, v22\n" \
"v_add_f32 v16, v16, v17\n" \
"v_mov_b32 v22, v21\n" \
"v_mul_f32 v15, v15, v20\n" \
"s_nop 0\n" \
"v_permlane16_swap_b32 v21, v22\n" \
"v_fma_f32 v21, v16, v20, v21\n" \
"v_add_f32 v21, v21, v22\n" \
"v_rcp_f32 v21, v21\n" \
"s_mov_b64 exec, s[32:33]\n" \
"v_mul_f32 v14, v14, v21\n" \
"v_mul_f32 v16, v15, v21\n" \
"v_pk_mul_f32 v[40:41], v[40:41], v[20:21] op_sel:[0,1] op_sel_hi:[1,1]\n" \
"s_waitcnt lgkmcnt(1)\n" \
"v_pk_mul_f32 v[0:1], v[0:1], v[14:15] op_sel_hi:[1,0]\n" \
"v_pk_mul_f32 v[2:3], v[2:3], v[14:15] op_sel_hi:[1,0]\n" \
"s_waitcnt lgkmcnt(0)\n" \
"v_pk_mul_f32 v[4:5], v[4:5], v[16:17] op_sel_hi:[1,0]\n" \
"v_pk_mul_f32 v[6:7], v[6:7], v[16:17] op_sel_hi:[1,0]\n" \
"v_pk_mul_f32 v[42:43], v[42:43], v[20:21] op_sel:[0,1] op_sel_hi:[1,1]\n" \
"v_cvt_pk_bf16_f32 v24, v0, v1\n" \
"v_cvt_pk_bf16_f32 v25, v2, v3\n" \
"v_cvt_pk_bf16_f32 v26, v4, v5\n" \
"v_cvt_pk_bf16_f32 v27, v6, v7\n" \
"buffer_store_dwordx4 v[24:27], v120, s[8:11], s24 offen sc1\n" \
"v_cvt_pk_bf16_f32 v28, v40, v41\n" \
"v_cvt_pk_bf16_f32 v29, v42, v43\n" \
"s_mov_b64 exec, s[22:23]\n" \
"buffer_store_dwordx2 v[28:29], v121, s[8:11], s24 offen sc1\n" \
"s_mov_b64 exec, -1\n" \
"s_add_u32 s34, s21, 4096\n" \
"v_lshrrev_b32 v30, 7, v126\n" \
"v_and_b32 v31, 0x7f, v126\n" \
"v_lshl_add_u32 v30, v30, 14, v31\n" \
"buffer_load_dwordx2 v[32:33], v30, s[8:11], s34 offen offset:3584 sc1\n" \
"s_waitcnt vmcnt(0)\n" \
"s_cmp_eq_u32 s18, 3\n" \
"s_cbranch_scc1 LS0_dec\n" \
"LS0_dec_soft:\n" \
"v_max_i32 v32, v32, v33\n" \
"s_nop 1\n" \
"v_max_i32_dpp v32, v32, v32 row_shr:1 row_mask:0xf bank_mask:0xf\n" \
"s_nop 1\n" \
"v_max_i32_dpp v32, v32, v32 row_shr:2 row_mask:0xf bank_mask:0xf\n" \
"s_nop 1\n" \
"v_max_i32_dpp v32, v32, v32 row_shr:4 row_mask:0xf bank_mask:0xf\n" \
"s_nop 1\n" \
"v_max_i32_dpp v32, v32, v32 row_shr:8 row_mask:0xf bank_mask:0xf\n" \
"s_nop 0\n" \
"v_readlane_b32 s62, v32, 15\n" \
"v_readlane_b32 s63, v32, 31\n" \
"v_readlane_b32 s38, v32, 47\n" \
"v_readlane_b32 s39, v32, 63\n" \
"s_max_i32 s62, s62, s63\n" \
"s_max_i32 s38, s38, s39\n" \
"s_max_i32 s62, s62, s38\n" \
"s_add_u32 s62, s62, s31\n" \
"s_add_u32 s18, s18, 1\n" \
"s_cmp_eq_u32 s18, 40\n" \
"s_cselect_b32 s34, 4, 0\n" \
"s_sub_u32 s31, s31, s34\n" \
"s_cmp_eq_u32 s18, 64\n" \
"s_cbranch_scc1 LS_end\n" \
"s_cmp_eq_u32 s18, 32\n" \
"s_cselect_b32 s35, s14, 0\n" \
"s_cmp_eq_u32 s18, 5\n" \
"s_cselect_b32 s35, s15, s35\n" \
"s_add_u32 s35, s35, 65536\n" \
"s_add_u32 s21, s21, s35\n" \
"s_add_u32 s24, s24, s35\n" \
"s_add_u32 s27, s27, 2048\n" \
"s_branch LS0_loop\n" \
"LS0_qr:\n" \
"s_cmp_lg_u32 s19, 0\n" \
"s_cbranch_scc1 LS0_q_soft\n" \
"s_waitcnt lgkmcnt(0)\n" \
"s_mov_b64 exec, vcc\n" \
"buffer_load_dwordx4 v[0:3], v112, s[8:11], s21 offen sc1\n" \
"buffer_load_dwordx4 v[4:7], v112, s[8:11], s21 offen offset:1024 sc1\n" \
"buffer_load_dwordx2 v[8:9], v113, s[8:11], s21 offen sc1\n" \
"s_mov_b64 exec, -1\n" \
"s_sub_u32 s20, s20, 1\n" \
"s_cselect_b32 s19, 1, 0\n" \
"s_nop 7\n" \
"v_mov_b64 v[40:41], 0\n" \
"v_mov_b64 v[42:43], 0\n" \
"v_mov_b64 v[44:45], 0\n" \
"v_mov_b64 v[46:47], 0\n" \
"v_mov_b64 v[48:49], 0\n" \
"v_mov_b64 v[50:51], 0\n" \
"s_branch LS0_q\n" \
"LS0_dec:\n" \
"s_mov_b32 s20, 262144\n" \
"LS0_dl:\n" \
"s_nop 1\n" \
"v_readlane_b32 s35, v32, 8\n" \
"s_cmp_lg_u32 s35, -1\n" \
"s_cbranch_scc1 LS0_dok\n" \
"s_cmp_lg_u32 s19, 0\n" \
"s_cbranch_scc1 LS0_dok\n" \
"s_sleep 4\n" \
"buffer_load_dwordx2 v[32:33], v30, s[8:11], s34 offen offset:3584 sc1\n" \
"s_sub_u32 s20, s20, 1\n" \
"s_cselect_b32 s19, 1, 0\n" \
"s_waitcnt vmcnt(0)\n" \
"s_branch LS0_dl\n" \
"LS0_dok:\n" \
"s_and_b32 s15, s35, 1\n" \
"s_lshl_b32 s15, s15, 13\n" \
"s_and_b32 s14, s35, 2\n" \
"s_lshl_b32 s14, s14, 12\n" \
"s_sub_u32 s14, s14, s15\n" \
"s_branch LS0_dec_soft\n" \
"LS1:\n" \
"s_branch LS1_go\n" \
".p2align 6\n" \
"LS1_loop:\n" \
"s_waitcnt lgkmcnt(0)\n" \
"LS1_tm:\n" \
"s_memrealtime s[38:39]\n" \
"s_waitcnt lgkmcnt(0)\n" \
"s_sub_u32 s38, s38, s62\n" \
"s_cmp_lt_i32 s38, 0\n" \
"s_cbranch_scc1 LS1_tm\n" \
"LS1_go:\n" \
"buffer_load_dwordx4 v[12:15], v112, s[8:11], s21 offen sc1\n" \
"buffer_load_dwordx4 v[16:19], v112, s[8:11], s21 offen offset:1024 sc1\n" \
"buffer_load_dwordx2 v[8:9], v113, s[8:11], s21 offen sc1\n" \
"ds_read_b128 v[64:67], v115 offset:0\n" \
"ds_read_b128 v[68:71], v115 offset:1024\n" \
"ds_read_b128 v[72:75], v115 offset:2048\n" \
"ds_read_b128 v[76:79], v115 offset:3072\n" \
"ds_read_b128 v[80:83], v115 offset:4096\n" \
"ds_read_b128 v[84:87], v115 offset:5120\n" \
"ds_read_b128 v[88:91], v115 offset:12288\n" \
"ds_read_b128 v[92:95], v115 offset:13312\n" \
"ds_read_b128 v[96:99], v115 offset:14336\n" \
"ds_read_b128 v[100:103], v115 offset:15360\n" \
"ds_read_b128 v[104:107], v115 offset:16384\n" \
"ds_read_b128 v[108:111], v115 offset:17408\n" \
"v_mov_b64 v[40:41], 0\n" \
"v_mov_b64 v[42:43], 0\n" \
"v_mov_b64 v[44:45], 0\n" \
"v_mov_b64 v[46:47], 0\n" \
"v_mov_b64 v[48:49], 0\n" \
"v_mov_b64 v[50:51], 0\n" \
"v_mov_b64 v[52:53], 0\n" \
"v_mov_b64 v[54:55], 0\n" \
"v_mov_b64 v[56:57], 0\n" \
"v_mov_b64 v[58:59], 0\n" \
"v_mov_b64 v[60:61], 0\n" \
"v_mov_b64 v[62:63], 0\n" \
"s_waitcnt lgkmcnt(0)\n" \
"s_mov_b32 s20, 262144\n" \
"LS1_q:\n" \
"s_waitcnt vmcnt(2)\n" \
"ds_write_b128 v124, v[12:15] offset:0\n" \
"v_smfmac_f32_16x16x64_bf16 v[40:43], v[12:15], a[96:103], v116\n" \
"s_waitcnt vmcnt(1)\n" \
"ds_write_b128 v124, v[16:19] offset:1024\n" \
"v_max3_u32 v122, v12, v13, v14\n" \
"v_max3_u32 v122, v122, v15, v16\n" \
"v_smfmac_f32_16x16x64_bf16 v[44:47], v[12:15], a[104:111], v116\n" \
"s_waitcnt vmcnt(0)\n" \
"ds_write_b64 v125, v[8:9] offset:0\n" \
"v_max3_u32 v122, v122, v17, v18\n" \
"v_max3_u32 v122, v122, v19, v8\n" \
"v_smfmac_f32_16x16x64_bf16 v[48:51], v[12:15], a[112:119], v116\n" \
"v_max_u32 v122, v122, v9\n" \
"v_cmp_eq_u32 vcc, -1, v122\n" \
"s_cbranch_vccnz LS1_qr\n" \
"LS1_q_soft:\n" \
"s_waitcnt lgkmcnt(0)\n" \
"s_barrier\n" \
"v_smfmac_f32_16x16x64_bf16 v[40:43], v[16:19], a[144:151], v116\n" \
"ds_read_b128 v[8:11], v114 offset:2048\n" \
"v_smfmac_f32_16x16x64_bf16 v[44:47], v[16:19], a[152:159], v116\n" \
"ds_read_b128 v[28:31], v114 offset:7168\n" \
"v_smfmac_f32_16x16x64_bf16 v[48:51], v[16:19], a[160:167], v116\n" \
"ds_read_b128 v[0:3], v114 offset:0\n" \
"v_smfmac_f32_16x16x64_bf16 v[52:55], v[12:15], a[120:127], v116\n" \
"ds_read_b128 v[4:7], v114 offset:1024\n" \
"v_smfmac_f32_16x16x64_bf16 v[56:59], v[12:15], a[128:135], v116\n" \
"ds_read_b128 v[20:23], v114 offset:5120\n" \
"v_smfmac_f32_16x16x64_bf16 v[60:63], v[12:15], a[136:143], v116\n" \
"ds_read_b128 v[24:27], v114 offset:6144\n" \
"v_smfmac_f32_16x16x64_bf16 v[52:55], v[16:19], a[168:175], v116\n" \
"ds_read_b128 v[32:35], v114 offset:8192\n" \
"v_smfmac_f32_16x16x64_bf16 v[56:59], v[16:19], a[176:183], v116\n" \
"ds_read_b128 v[36:39], v114 offset:9216\n" \
"v_smfmac_f32_16x16x64_bf16 v[60:63], v[16:19], a[184:191], v116\n" \
"s_waitcnt lgkmcnt(7)\n" \
"v_smfmac_f32_16x16x64_bf16 v[40:43], v[8:11], v[64:71], v116\n" \
"v_smfmac_f32_16x16x64_bf16 v[44:47], v[8:11], v[72:79], v116\n" \
"v_smfmac_f32_16x16x64_bf16 v[48:51], v[8:11], v[80:87], v116\n" \
"s_waitcnt lgkmcnt(6)\n" \
"v_smfmac_f32_16x16x64_bf16 v[40:43], v[28:31], v[88:95], v116\n" \
"v_smfmac_f32_16x16x64_bf16 v[44:47], v[28:31], v[96:103], v116\n" \
"v_smfmac_f32_16x16x64_bf16 v[48:51], v[28:31], v[104:111], v116\n" \
"s_waitcnt lgkmcnt(5)\n" \
"v_smfmac_f32_16x16x64_bf16 v[40:43], v[0:3], a[0:7], v116\n" \
"s_waitcnt lgkmcnt(4)\n" \
"v_smfmac_f32_16x16x64_bf16 v[40:43], v[4:7], a[48:55], v116\n" \
"s_waitcnt lgkmcnt(3)\n" \
"v_smfmac_f32_16x16x64_bf16 v[40:43], v[20:23], a[192:199], v116\n" \
"ds_read_b128 v[64:67], v115 offset:6144\n" \
"s_waitcnt lgkmcnt(3)\n" \
"v_smfmac_f32_16x16x64_bf16 v[40:43], v[24:27], a[240:247], v116\n" \
"ds_read_b128 v[68:71], v115 offset:7168\n" \
"s_waitcnt lgkmcnt(3)\n" \
"v_smfmac_f32_16x16x64_bf16 v[40:43], v[32:35], v[160:167], v116\n" \
"ds_read_b128 v[72:75], v115 offset:8192\n" \
"s_waitcnt lgkmcnt(3)\n" \
"v_smfmac_f32_16x16x64_bf16 v[40:43], v[36:39], v[208:215], v116\n" \
"ds_read_b128 v[76:79], v115 offset:9216\n" \
"v_smfmac_f32_16x16x64_bf16 v[44:47], v[0:3], a[8:15], v116\n" \
"ds_read_b128 v[80:83], v115 offset:10240\n" \
"v_smfmac_f32_16x16x64_bf16 v[44:47], v[4:7], a[56:63], v116\n" \
"ds_read_b128 v[84:87], v115 offset:11264\n" \
"v_smfmac_f32_16x16x64_bf16 v[44:47], v[20:23], a[200:207], v116\n" \
"ds_read_b128 v[88:91], v115 offset:18432\n" \
"v_permlane32_swap_b32 v40, v42\n" \
"v_smfmac_f32_16x16x64_bf16 v[44:47], v[24:27], a[248:255], v116\n" \
"ds_read_b128 v[92:95], v115 offset:19456\n" \
"v_permlane32_swap_b32 v41, v43\n" \
"v_smfmac_f32_16x16x64_bf16 v[44:47], v[32:35], v[168:175], v116\n" \
"ds_read_b128 v[96:99], v115 offset:20480\n" \
"v_add_f32 v40, v40, v42\n" \
"v_smfmac_f32_16x16x64_bf16 v[44:47], v[36:39], v[216:223], v116\n" \
"ds_read_b128 v[100:103], v115 offset:21504\n" \
"v_add_f32 v41, v41, v43\n" \
"v_smfmac_f32_16x16x64_bf16 v[48:51], v[0:3], a[16:23], v116\n" \
"ds_read_b128 v[104:107], v115 offset:22528\n" \
"v_cvt_pk_bf16_f32 v122, v40, v41\n" \
"v_smfmac_f32_16x16x64_bf16 v[48:51], v[4:7], a[64:71], v116\n" \
"ds_write_b32 v117, v122 offset:0\n" \
"v_smfmac_f32_16x16x64_bf16 v[48:51], v[20:23], a[208:215], v116\n" \
"ds_read_b128 v[108:111], v115 offset:23552\n" \
"v_permlane32_swap_b32 v44, v46\n" \
"v_smfmac_f32_16x16x64_bf16 v[48:51], v[24:27], v[128:135], v116\n" \
"v_permlane32_swap_b32 v45, v47\n" \
"v_smfmac_f32_16x16x64_bf16 v[48:51], v[32:35], v[176:183], v116\n" \
"v_add_f32 v44, v44, v46\n" \
"v_smfmac_f32_16x16x64_bf16 v[48:51], v[36:39], v[224:231], v116\n" \
"v_add_f32 v45, v45, v47\n" \
"s_waitcnt lgkmcnt(11)\n" \
"v_smfmac_f32_16x16x64_bf16 v[52:55], v[8:11], v[64:71], v116\n" \
"v_cvt_pk_bf16_f32 v123, v44, v45\n" \
"s_waitcnt lgkmcnt(9)\n" \
"v_smfmac_f32_16x16x64_bf16 v[56:59], v[8:11], v[72:79], v116\n" \
"ds_write_b32 v117, v123 offset:512\n" \
"s_waitcnt lgkmcnt(8)\n" \
"v_smfmac_f32_16x16x64_bf16 v[60:63], v[8:11], v[80:87], v116\n" \
"ds_read_b128 v[64:67], v127 offset:0\n" \
"v_permlane32_swap_b32 v48, v50\n" \
"s_waitcnt lgkmcnt(7)\n" \
"v_smfmac_f32_16x16x64_bf16 v[52:55], v[28:31], v[88:95], v116\n" \
"ds_read_b64 v[68:69], v127 offset:1024\n" \
"v_permlane32_swap_b32 v49, v51\n" \
"s_waitcnt lgkmcnt(6)\n" \
"v_smfmac_f32_16x16x64_bf16 v[56:59], v[28:31], v[96:103], v116\n" \
"ds_read_b64 v[70:71], v127 offset:7168\n" \
"v_add_f32 v48, v48, v50\n" \
"s_waitcnt lgkmcnt(4)\n" \
"v_smfmac_f32_16x16x64_bf16 v[60:63], v[28:31], v[104:111], v116\n" \
"ds_read_b128 v[72:75], v127 offset:2048\n" \
"v_add_f32 v49, v49, v51\n" \
"v_smfmac_f32_16x16x64_bf16 v[52:55], v[0:3], a[24:31], v116\n" \
"ds_read_b128 v[76:79], v127 offset:3072\n" \
"v_cvt_pk_bf16_f32 v122, v48, v49\n" \
"v_smfmac_f32_16x16x64_bf16 v[52:55], v[4:7], a[72:79], v116\n" \
"ds_write_b32 v117, v122 offset:1024\n" \
"v_smfmac_f32_16x16x64_bf16 v[52:55], v[20:23], a[216:223], v116\n" \
"ds_read_b64 v[80:81], v127 offset:4096\n" \
"s_waitcnt lgkmcnt(1)\n" \
"s_barrier\n" \
"v_smfmac_f32_16x16x64_bf16 v[52:55], v[24:27], v[136:143], v116\n" \
"ds_read_b64 v[82:83], v127 offset:10240\n" \
"ds_read_b64_tr_b16 v[88:89], v118 offset:0\n" \
"ds_read_b64_tr_b16 v[90:91], v118 offset:512\n" \
"v_smfmac_f32_16x16x64_bf16 v[52:55], v[32:35], v[184:191], v116\n" \
"ds_read_b128 v[84:87], v127 offset:5120\n" \
"ds_read_b64_tr_b16 v[92:93], v118 offset:1024\n" \
"ds_read_b64_tr_b16 v[94:95], v118 offset:1536\n" \
"v_smfmac_f32_16x16x64_bf16 v[52:55], v[36:39], v[232:239], v116\n" \
"ds_read_b64_tr_b16 v[96:97], v118 offset:2048\n" \
"ds_read_b64_tr_b16 v[98:99], v118 offset:2560\n" \
"v_smfmac_f32_16x16x64_bf16 v[56:59], v[0:3], a[32:39], v116\n" \
"ds_read_b64_tr_b16 v[100:101], v118 offset:3072\n" \
"ds_read_b64_tr_b16 v[102:103], v118 offset:3584\n" \
"v_smfmac_f32_16x16x64_bf16 v[56:59], v[4:7], a[80:87], v116\n" \
"ds_read_b64_tr_b16 v[104:105], v118 offset:4096\n" \
"ds_read_b64_tr_b16 v[106:107], v118 offset:4608\n" \
"v_smfmac_f32_16x16x64_bf16 v[56:59], v[20:23], a[224:231], v116\n" \
"ds_read_b64_tr_b16 v[108:109], v118 offset:5120\n" \
"ds_read_b64_tr_b16 v[110:111], v118 offset:5632\n" \
"v_permlane32_swap_b32 v52, v54\n" \
"v_smfmac_f32_16x16x64_bf16 v[56:59], v[24:27], v[144:151], v116\n" \
"v_permlane32_swap_b32 v53, v55\n" \
"v_smfmac_f32_16x16x64_bf16 v[56:59], v[32:35], v[192:199], v116\n" \
"v_add_f32 v52, v52, v54\n" \
"v_smfmac_f32_16x16x64_bf16 v[56:59], v[36:39], v[240:247], v116\n" \
"v_add_f32 v53, v53, v55\n" \
"s_waitcnt lgkmcnt(11)\n" \
"v_mfma_f32_16x16x32_bf16 v[40:43], v[64:67], v[88:91], 0\n" \
"v_cvt_pk_bf16_f32 v123, v52, v53\n" \
"s_waitcnt lgkmcnt(8)\n" \
"v_mfma_f32_16x16x32_bf16 v[40:43], v[68:71], v[92:95], v[40:43]\n" \
"ds_write_b32 v117, v123 offset:16\n" \
"s_waitcnt lgkmcnt(7)\n" \
"v_mfma_f32_16x16x32_bf16 v[40:43], v[72:75], v[96:99], v[40:43]\n" \
"v_permlane32_swap_b32 v56, v58\n" \
"v_permlane32_swap_b32 v57, v59\n" \
"s_waitcnt lgkmcnt(5)\n" \
"v_mfma_f32_16x16x32_bf16 v[40:43], v[76:79], v[100:103], v[40:43]\n" \
"v_add_f32 v56, v56, v58\n" \
"v_add_f32 v57, v57, v59\n" \
"s_waitcnt lgkmcnt(3)\n" \
"v_mfma_f32_16x16x32_bf16 v[40:43], v[80:83], v[104:107], v[40:43]\n" \
"v_cvt_pk_bf16_f32 v122, v56, v57\n" \
"s_waitcnt lgkmcnt(1)\n" \
"v_mfma_f32_16x16x32_bf16 v[40:43], v[84:87], v[108:111], v[40:43]\n" \
"ds_write_b32 v117, v122 offset:528\n" \
"v_smfmac_f32_16x16x64_bf16 v[60:63], v[0:3], a[40:47], v116\n" \
"v_smfmac_f32_16x16x64_bf16 v[60:63], v[4:7], a[88:95], v116\n" \
"v_smfmac_f32_16x16x64_bf16 v[60:63], v[20:23], a[232:239], v116\n" \
"v_max3_f32 v44, v40, v41, v42\n" \
"v_smfmac_f32_16x16x64_bf16 v[60:63], v[24:27], v[152:159], v116\n" \
"v_max_f32 v44, v44, v43\n" \
"v_smfmac_f32_16x16x64_bf16 v[60:63], v[32:35], v[200:207], v116\n" \
"v_mov_b32 v45, v44\n" \
"v_smfmac_f32_16x16x64_bf16 v[60:63], v[36:39], v[248:255], v116\n" \
"s_nop 0\n" \
"v_permlane16_swap_b32 v44, v45\n" \
"v_max_f32 v44, v44, v45\n" \
"v_mov_b32 v45, v44\n" \
"s_nop 3\n" \
"v_permlane32_swap_b32 v60, v62\n" \
"v_permlane32_swap_b32 v61, v63\n" \
"v_add_f32 v60, v60, v62\n" \
"v_add_f32 v61, v61, v63\n" \
"v_cvt_pk_bf16_f32 v123, v60, v61\n" \
"ds_write_b32 v117, v123 offset:1040\n" \
"s_waitcnt lgkmcnt(0)\n" \
"s_barrier\n" \
"v_permlane32_swap_b32 v44, v45\n" \
"v_max_f32 v44, v44, v45\n" \
"v_mul_f32 v46, s30, v44\n" \
"v_fma_f32 v40, v40, s30, -v46\n" \
"v_fma_f32 v41, v41, s30, -v46\n" \
"v_fma_f32 v42, v42, s30, -v46\n" \
"v_fma_f32 v43, v43, s30, -v46\n" \
"v_exp_f32 v40, v40\n" \
"v_exp_f32 v41, v41\n" \
"v_exp_f32 v42, v42\n" \
"v_exp_f32 v43, v43\n" \
"v_add_f32 v45, v40, v41\n" \
"v_add_f32 v46, v42, v43\n" \
"v_add_f32 v45, v45, v46\n" \
"ds_write_b128 v114, v[40:43] offset:40960\n" \
"ds_write_b64 v126, v[44:45] offset:43008\n" \
"s_waitcnt lgkmcnt(0)\n" \
"s_barrier\n" \
"s_add_u32 s34, s21, 4096\n" \
"v_lshrrev_b32 v60, 7, v126\n" \
"v_and_b32 v61, 0x7f, v126\n" \
"v_lshl_add_u32 v60, v60, 14, v61\n" \
"buffer_load_dwordx2 v[122:123], v60, s[8:11], s34 offen offset:3584 sc1\n" \
"buffer_load_dwordx4 v[0:3], v114, s[8:11], s26 offen offset:0 sc1\n" \
"buffer_load_dwordx2 v[4:5], v114, s[8:11], s26 offen offset:1024 sc1\n" \
"s_add_u32 s35, s26, 4096\n" \
"buffer_load_dwordx2 v[6:7], v114, s[8:11], s35 offen offset:3072 sc1\n" \
"buffer_load_dwordx4 v[8:11], v114, s[8:11], s26 offen offset:2048 sc1\n" \
"buffer_load_dwordx4 v[12:15], v114, s[8:11], s26 offen offset:3072 sc1\n" \
"buffer_load_dwordx2 v[16:17], v114, s[8:11], s35 offen offset:0 sc1\n" \
"s_add_u32 s35, s26, 8192\n" \
"buffer_load_dwordx2 v[18:19], v114, s[8:11], s35 offen offset:2048 sc1\n" \
"s_add_u32 s35, s26, 4096\n" \
"buffer_load_dwordx4 v[20:23], v114, s[8:11], s35 offen offset:1024 sc1\n" \
"s_add_u32 s26, s26, 12288\n" \
"s_sleep 2\n" \
"ds_read_b128 v[24:27], v127 offset:6144\n" \
"ds_read_b64 v[28:29], v127 offset:1032\n" \
"ds_read_b64 v[30:31], v127 offset:7176\n" \
"ds_read_b128 v[32:35], v127 offset:8192\n" \
"ds_read_b128 v[36:39], v127 offset:9216\n" \
"ds_read_b64 v[40:41], v127 offset:4104\n" \
"ds_read_b64 v[42:43], v127 offset:10248\n" \
"ds_read_b128 v[44:47], v127 offset:11264\n" \
"ds_read_b64_tr_b16 v[88:89], v118 offset:0\n" \
"ds_read_b64_tr_b16 v[90:91], v118 offset:512\n" \
"ds_read_b64_tr_b16 v[92:93], v118 offset:1024\n" \
"ds_read_b64_tr_b16 v[94:95], v118 offset:1536\n" \
"ds_read_b64_tr_b16 v[96:97], v118 offset:2048\n" \
"ds_read_b64_tr_b16 v[98:99], v118 offset:2560\n" \
"ds_read_b64_tr_b16 v[100:101], v118 offset:3072\n" \
"ds_read_b64_tr_b16 v[102:103], v118 offset:3584\n" \
"ds_read_b64_tr_b16 v[104:105], v118 offset:4096\n" \
"ds_read_b64_tr_b16 v[106:107], v118 offset:4608\n" \
"ds_read_b64_tr_b16 v[108:109], v118 offset:5120\n" \
"ds_read_b64_tr_b16 v[110:111], v118 offset:5632\n" \
"v_mov_b32 v60, 0xff61b1e6\n" \
"s_waitcnt vmcnt(7)\n" \
"v_mfma_f32_16x16x32_bf16 v[48:51], v[64:67], v[0:3], 0\n" \
"s_waitcnt lgkmcnt(15)\n" \
"v_mfma_f32_16x16x32_bf16 v[52:55], v[24:27], v[0:3], 0\n" \
"s_waitcnt lgkmcnt(10)\n" \
"v_mfma_f32_16x16x32_bf16 v[56:59], v[88:91], v[0:3], 0\n" \
"s_waitcnt vmcnt(5)\n" \
"v_mfma_f32_16x16x32_bf16 v[48:51], v[68:71], v[4:7], v[48:51]\n" \
"v_mfma_f32_16x16x32_bf16 v[52:55], v[28:31], v[4:7], v[52:55]\n" \
"s_waitcnt lgkmcnt(8)\n" \
"v_mfma_f32_16x16x32_bf16 v[56:59], v[92:95], v[4:7], v[56:59]\n" \
"s_waitcnt vmcnt(4)\n" \
"v_mfma_f32_16x16x32_bf16 v[48:51], v[72:75], v[8:11], v[48:51]\n" \
"v_mfma_f32_16x16x32_bf16 v[52:55], v[32:35], v[8:11], v[52:55]\n" \
"s_waitcnt lgkmcnt(6)\n" \
"v_mfma_f32_16x16x32_bf16 v[56:59], v[96:99], v[8:11], v[56:59]\n" \
"s_waitcnt vmcnt(3)\n" \
"v_mfma_f32_16x16x32_bf16 v[48:51], v[76:79], v[12:15], v[48:51]\n" \
"v_mfma_f32_16x16x32_bf16 v[52:55], v[36:39], v[12:15], v[52:55]\n" \
"s_waitcnt lgkmcnt(4)\n" \
"v_mfma_f32_16x16x32_bf16 v[56:59], v[100:103], v[12:15], v[56:59]\n" \
"s_waitcnt vmcnt(1)\n" \
"v_mfma_f32_16x16x32_bf16 v[48:51], v[80:83], v[16:19], v[48:51]\n" \
"v_mfma_f32_16x16x32_bf16 v[52:55], v[40:43], v[16:19], v[52:55]\n" \
"s_waitcnt lgkmcnt(2)\n" \
"v_mfma_f32_16x16x32_bf16 v[56:59], v[104:107], v[16:19], v[56:59]\n" \
"s_waitcnt vmcnt(0)\n" \
"v_mfma_f32_16x16x32_bf16 v[48:51], v[84:87], v[20:23], v[48:51]\n" \
"v_mfma_f32_16x16x32_bf16 v[52:55], v[44:47], v[20:23], v[52:55]\n" \
"s_waitcnt lgkmcnt(0)\n" \
"v_mfma_f32_16x16x32_bf16 v[56:59], v[108:111], v[20:23], v[56:59]\n" \
"s_nop 1\n" \
"v_cndmask_b32 v48, v60, v48, s[64:65]\n" \
"v_cndmask_b32 v49, v60, v49, s[66:67]\n" \
"v_cndmask_b32 v50, v60, v50, s[68:69]\n" \
"v_cndmask_b32 v51, v60, v51, s[70:71]\n" \
"v_cndmask_b32 v52, v60, v52, s[72:73]\n" \
"v_cndmask_b32 v53, v60, v53, s[74:75]\n" \
"v_cndmask_b32 v54, v60, v54, s[76:77]\n" \
"v_cndmask_b32 v55, v60, v55, s[78:79]\n" \
"v_cndmask_b32 v56, v60, v56, s[16:17]\n" \
"v_cndmask_b32 v57, v60, v57, s[16:17]\n" \
"v_cndmask_b32 v58, v60, v58, s[16:17]\n" \
"v_cndmask_b32 v59, v60, v59, s[16:17]\n" \
"v_max3_f32 v61, v48, v49, v50\n" \
"v_max3_f32 v62, v51, v52, v53\n" \
"v_max3_f32 v61, v61, v54, v55\n" \
"v_max3_f32 v62, v62, v56, v57\n" \
"v_max3_f32 v61, v61, v58, v59\n" \
"v_max_f32 v61, v61, v62\n" \
"v_mov_b32 v62, v61\n" \
"s_nop 1\n" \
"v_permlane16_swap_b32 v61, v62\n" \
"v_max_f32 v61, v61, v62\n" \
"v_mov_b32 v62, v61\n" \
"s_nop 1\n" \
"v_permlane32_swap_b32 v61, v62\n" \
"v_max_f32 v61, v61, v62\n" \
"v_mul_f32 v61, s30, v61\n" \
"v_fma_f32 v48, v48, s30, -v61\n" \
"v_fma_f32 v49, v49, s30, -v61\n" \
"v_fma_f32 v50, v50, s30, -v61\n" \
"v_fma_f32 v51, v51, s30, -v61\n" \
"v_fma_f32 v52, v52, s30, -v61\n" \
"v_fma_f32 v53, v53, s30, -v61\n" \
"v_fma_f32 v54, v54, s30, -v61\n" \
"v_fma_f32 v55, v55, s30, -v61\n" \
"v_fma_f32 v56, v56, s30, -v61\n" \
"v_fma_f32 v57, v57, s30, -v61\n" \
"v_fma_f32 v58, v58, s30, -v61\n" \
"v_fma_f32 v59, v59, s30, -v61\n" \
"v_exp_f32 v48, v48\n" \
"v_exp_f32 v49, v49\n" \
"v_exp_f32 v50, v50\n" \
"v_exp_f32 v51, v51\n" \
"v_exp_f32 v52, v52\n" \
"v_exp_f32 v53, v53\n" \
"v_exp_f32 v54, v54\n" \
"v_exp_f32 v55, v55\n" \
"v_exp_f32 v56, v56\n" \
"v_exp_f32 v57, v57\n" \
"v_exp_f32 v58, v58\n" \
"v_exp_f32 v59, v59\n" \
"v_add_f32 v61, v48, v49\n" \
"v_add_f32 v62, v50, v51\n" \
"v_add_f32 v61, v61, v52\n" \
"v_add_f32 v62, v62, v53\n" \
"v_add_f32 v61, v61, v54\n" \
"v_add_f32 v62, v62, v55\n" \
"v_add_f32 v61, v61, v56\n" \
"v_add_f32 v62, v62, v57\n" \
"v_add_f32 v61, v61, v58\n" \
"v_add_f32 v62, v62, v59\n" \
"v_add_f32 v61, v61, v62\n" \
"v_mov_b32 v62, v61\n" \
"s_nop 1\n" \
"v_permlane16_swap_b32 v61, v62\n" \
"v_add_f32 v61, v61, v62\n" \
"v_mov_b32 v62, v61\n" \
"s_nop 1\n" \
"v_permlane32_swap_b32 v61, v62\n" \
"v_add_f32 v61, v61, v62\n" \
"v_rcp_f32 v61, v61\n" \
"s_nop 0\n" \
"v_mul_f32 v48, v48, v61\n" \
"v_mul_f32 v49, v49, v61\n" \
"v_mul_f32 v50, v50, v61\n" \
"v_mul_f32 v51, v51, v61\n" \
"v_mul_f32 v52, v52, v61\n" \
"v_mul_f32 v53, v53, v61\n" \
"v_mul_f32 v54, v54, v61\n" \
"v_mul_f32 v55, v55, v61\n" \
"v_mul_f32 v56, v56, v61\n" \
"v_mul_f32 v57, v57, v61\n" \
"v_mul_f32 v58, v58, v61\n" \
"v_mul_f32 v59, v59, v61\n" \
"v_cvt_pk_bf16_f32 v24, v48, v49\n" \
"v_cvt_pk_bf16_f32 v25, v50, v51\n" \
"v_cvt_pk_bf16_f32 v26, v52, v53\n" \
"v_cvt_pk_bf16_f32 v27, v54, v55\n" \
"v_cvt_pk_bf16_f32 v28, v56, v57\n" \
"v_cvt_pk_bf16_f32 v29, v58, v59\n" \
"buffer_store_dwordx4 v[24:27], v114, s[8:11], s25 offen sc1\n" \
"s_mov_b64 exec, s[16:17]\n" \
"buffer_store_dwordx2 v[28:29], v126, s[8:11], s25 offen offset:768 sc1\n" \
"s_mov_b64 exec, -1\n" \
"s_add_u32 s25, s25, 81920\n" \
"v_xor_b32 v127, 12288, v127\n" \
"s_cmp_eq_u32 s18, 3\n" \
"s_cbranch_scc1 LS1_dec\n" \
"LS1_dec_soft:\n" \
"v_max_i32 v122, v122, v123\n" \
"s_nop 1\n" \
"v_max_i32_dpp v122, v122, v122 row_shr:1 row_mask:0xf bank_mask:0xf\n" \
"s_nop 1\n" \
"v_max_i32_dpp v122, v122, v122 row_shr:2 row_mask:0xf bank_mask:0xf\n" \
"s_nop 1\n" \
"v_max_i32_dpp v122, v122, v122 row_shr:4 row_mask:0xf bank_mask:0xf\n" \
"s_nop 1\n" \
"v_max_i32_dpp v122, v122, v122 row_shr:8 row_mask:0xf bank_mask:0xf\n" \
"s_nop 0\n" \
"v_readlane_b32 s62, v122, 15\n" \
"v_readlane_b32 s63, v122, 31\n" \
"v_readlane_b32 s38, v122, 47\n" \
"v_readlane_b32 s39, v122, 63\n" \
"s_max_i32 s62, s62, s63\n" \
"s_max_i32 s38, s38, s39\n" \
"s_max_i32 s62, s62, s38\n" \
"s_add_u32 s62, s62, s31\n" \
"s_add_u32 s18, s18, 1\n" \
"s_cmp_eq_u32 s18, 40\n" \
"s_cselect_b32 s34, 4, 0\n" \
"s_sub_u32 s31, s31, s34\n" \
"s_cmp_eq_u32 s18, 64\n" \
"s_cbranch_scc1 LS_end\n" \
"s_cmp_eq_u32 s18, 32\n" \
"s_cselect_b32 s35, s14, 0\n" \
"s_cmp_eq_u32 s18, 5\n" \
"s_cselect_b32 s35, s15, s35\n" \
"s_add_u32 s35, s35, 65536\n" \
"s_add_u32 s21, s21, s35\n" \
"s_add_u32 s24, s24, s35\n" \
"s_add_u32 s27, s27, 2048\n" \
"s_branch LS1_loop\n" \
"LS1_qr:\n" \
"s_cmp_lg_u32 s19, 0\n" \
"s_cbranch_scc1 LS1_q_soft\n" \
"s_waitcnt lgkmcnt(0)\n" \
"s_mov_b64 exec, vcc\n" \
"buffer_load_dwordx4 v[12:15], v112, s[8:11], s21 offen sc1\n" \
"buffer_load_dwordx4 v[16:19], v112, s[8:11], s21 offen offset:1024 sc1\n" \
"buffer_load_dwordx2 v[8:9], v113, s[8:11], s21 offen sc1\n" \
"s_mov_b64 exec, -1\n" \
"s_sub_u32 s20, s20, 1\n" \
"s_cselect_b32 s19, 1, 0\n" \
"s_nop 7\n" \
"v_mov_b64 v[40:41], 0\n" \
"v_mov_b64 v[42:43], 0\n" \
"v_mov_b64 v[44:45], 0\n" \
"v_mov_b64 v[46:47], 0\n" \
"v_mov_b64 v[48:49], 0\n" \
"v_mov_b64 v[50:51], 0\n" \
"s_branch LS1_q\n" \
"LS1_dec:\n" \
"s_mov_b32 s20, 262144\n" \
"v_lshrrev_b32 v60, 7, v126\n" \
"v_and_b32 v61, 0x7f, v126\n" \
"v_lshl_add_u32 v60, v60, 14, v61\n" \
"LS1_dl:\n" \
"s_nop 1\n" \
"v_readlane_b32 s35, v122, 8\n" \
"s_cmp_lg_u32 s35, -1\n" \
"s_cbranch_scc1 LS1_dok\n" \
"s_cmp_lg_u32 s19, 0\n" \
"s_cbranch_scc1 LS1_dok\n" \
"s_sleep 4\n" \
"buffer_load_dwordx2 v[122:123], v60, s[8:11], s34 offen offset:3584 sc1\n" \
"s_sub_u32 s20, s20, 1\n" \
"s_cselect_b32 s19, 1, 0\n" \
"s_waitcnt vmcnt(0)\n" \
"s_branch LS1_dl\n" \
"LS1_dok:\n" \
"s_and_b32 s15, s35, 1\n" \
"s_lshl_b32 s15, s15, 13\n" \
"s_and_b32 s14, s35, 2\n" \
"s_lshl_b32 s14, s14, 12\n" \
"s_sub_u32 s14, s14, s15\n" \
"s_branch LS1_dec_soft\n" \
"LS2:\n" \
"s_branch LS2_go\n" \
".p2align 6\n" \
"LS2_loop:\n" \
"s_waitcnt lgkmcnt(0)\n" \
"LS2_tm:\n" \
"s_memrealtime s[38:39]\n" \
"s_waitcnt lgkmcnt(0)\n" \
"s_sub_u32 s38, s38, s62\n" \
"s_cmp_lt_i32 s38, 0\n" \
"s_cbranch_scc1 LS2_tm\n" \
"LS2_go:\n" \
"buffer_load_dwordx4 v[20:23], v112, s[8:11], s21 offen sc1\n" \
"buffer_load_dwordx4 v[24:27], v112, s[8:11], s21 offen offset:1024 sc1\n" \
"buffer_load_dwordx2 v[28:29], v113, s[8:11], s21 offen sc1\n" \
"ds_read_b128 v[64:67], v115 offset:0\n" \
"ds_read_b128 v[68:71], v115 offset:1024\n" \
"ds_read_b128 v[72:75], v115 offset:2048\n" \
"ds_read_b128 v[76:79], v115 offset:3072\n" \
"ds_read_b128 v[80:83], v115 offset:4096\n" \
"ds_read_b128 v[84:87], v115 offset:5120\n" \
"ds_read_b128 v[88:91], v115 offset:12288\n" \
"ds_read_b128 v[92:95], v115 offset:13312\n" \
"ds_read_b128 v[96:99], v115 offset:14336\n" \
"ds_read_b128 v[100:103], v115 offset:15360\n" \
"ds_read_b128 v[104:107], v115 offset:16384\n" \
"ds_read_b128 v[108:111], v115 offset:17408\n" \
"v_mov_b64 v[40:41], 0\n" \
"v_mov_b64 v[42:43], 0\n" \
"v_mov_b64 v[44:45], 0\n" \
"v_mov_b64 v[46:47], 0\n" \
"v_mov_b64 v[48:49], 0\n" \
"v_mov_b64 v[50:51], 0\n" \
"v_mov_b64 v[52:53], 0\n" \
"v_mov_b64 v[54:55], 0\n" \
"v_mov_b64 v[56:57], 0\n" \
"v_mov_b64 v[58:59], 0\n" \
"v_mov_b64 v[60:61], 0\n" \
"v_mov_b64 v[62:63], 0\n" \
"s_waitcnt lgkmcnt(0)\n" \
"s_mov_b32 s20, 262144\n" \
"LS2_q:\n" \
"s_waitcnt vmcnt(2)\n" \
"ds_write_b128 v124, v[20:23] offset:0\n" \
"v_smfmac_f32_16x16x64_bf16 v[40:43], v[20:23], a[192:199], v116\n" \
"s_waitcnt vmcnt(1)\n" \
"ds_write_b128 v124, v[24:27] offset:1024\n" \
"v_max3_u32 v122, v20, v21, v22\n" \
"v_max3_u32 v122, v122, v23, v24\n" \
"v_smfmac_f32_16x16x64_bf16 v[44:47], v[20:23], a[200:207], v116\n" \
"s_waitcnt vmcnt(0)\n" \
"ds_write_b64 v125, v[28:29] offset:0\n" \
"v_max3_u32 v122, v122, v25, v26\n" \
"v_max3_u32 v122, v122, v27, v28\n" \
"v_smfmac_f32_16x16x64_bf16 v[48:51], v[20:23], a[208:215], v116\n" \
"v_max_u32 v122, v122, v29\n" \
"v_cmp_eq_u32 vcc, -1, v122\n" \
"s_cbranch_vccnz LS2_qr\n" \
"LS2_q_soft:\n" \
"s_waitcnt lgkmcnt(0)\n" \
"s_barrier\n" \
"v_smfmac_f32_16x16x64_bf16 v[40:43], v[24:27], a[240:247], v116\n" \
"ds_read_b128 v[8:11], v114 offset:2048\n" \
"v_smfmac_f32_16x16x64_bf16 v[44:47], v[24:27], a[248:255], v116\n" \
"ds_read_b128 v[28:31], v114 offset:7168\n" \
"v_smfmac_f32_16x16x64_bf16 v[48:51], v[24:27], v[128:135], v116\n" \
"ds_read_b128 v[0:3], v114 offset:0\n" \
"v_smfmac_f32_16x16x64_bf16 v[52:55], v[20:23], a[216:223], v116\n" \
"ds_read_b128 v[4:7], v114 offset:1024\n" \
"v_smfmac_f32_16x16x64_bf16 v[56:59], v[20:23], a[224:231], v116\n" \
"ds_read_b128 v[12:15], v114 offset:3072\n" \
"v_smfmac_f32_16x16x64_bf16 v[60:63], v[20:23], a[232:239], v116\n" \
"ds_read_b128 v[16:19], v114 offset:4096\n" \
"v_smfmac_f32_16x16x64_bf16 v[52:55], v[24:27], v[136:143], v116\n" \
"ds_read_b128 v[32:35], v114 offset:8192\n" \
"v_smfmac_f32_16x16x64_bf16 v[56:59], v[24:27], v[144:151], v116\n" \
"ds_read_b128 v[36:39], v114 offset:9216\n" \
"v_smfmac_f32_16x16x64_bf16 v[60:63], v[24:27], v[152:159], v116\n" \
"s_waitcnt lgkmcnt(7)\n" \
"v_smfmac_f32_16x16x64_bf16 v[40:43], v[8:11], v[64:71], v116\n" \
"v_smfmac_f32_16x16x64_bf16 v[44:47], v[8:11], v[72:79], v116\n" \
"v_smfmac_f32_16x16x64_bf16 v[48:51], v[8:11], v[80:87], v116\n" \
"s_waitcnt lgkmcnt(6)\n" \
"v_smfmac_f32_16x16x64_bf16 v[40:43], v[28:31], v[88:95], v116\n" \
"v_smfmac_f32_16x16x64_bf16 v[44:47], v[28:31], v[96:103], v116\n" \
"v_smfmac_f32_16x16x64_bf16 v[48:51], v[28:31], v[104:111], v116\n" \
"s_waitcnt lgkmcnt(5)\n" \
"v_smfmac_f32_16x16x64_bf16 v[40:43], v[0:3], a[0:7], v116\n" \
"s_waitcnt lgkmcnt(4)\n" \
"v_smfmac_f32_16x16x64_bf16 v[40:43], v[4:7], a[48:55], v116\n" \
"s_waitcnt lgkmcnt(3)\n" \
"v_smfmac_f32_16x16x64_bf16 v[40:43], v[12:15], a[96:103], v116\n" \
"ds_read_b128 v[64:67], v115 offset:6144\n" \
"s_waitcnt lgkmcnt(3)\n" \
"v_smfmac_f32_16x16x64_bf16 v[40:43], v[16:19], a[144:151], v116\n" \
"ds_read_b128 v[68:71], v115 offset:7168\n" \
"s_waitcnt lgkmcnt(3)\n" \
"v_smfmac_f32_16x16x64_bf16 v[40:43], v[32:35], v[160:167], v116\n" \
"ds_read_b128 v[72:75], v115 offset:8192\n" \
"s_waitcnt lgkmcnt(3)\n" \
"v_smfmac_f32_16x16x64_bf16 v[40:43], v[36:39], v[208:215], v116\n" \
"ds_read_b128 v[76:79], v115 offset:9216\n" \
"v_smfmac_f32_16x16x64_bf16 v[44:47], v[0:3], a[8:15], v116\n" \
"ds_read_b128 v[80:83], v115 offset:10240\n" \
"v_smfmac_f32_16x16x64_bf16 v[44:47], v[4:7], a[56:63], v116\n" \
"ds_read_b128 v[84:87], v115 offset:11264\n" \
"v_smfmac_f32_16x16x64_bf16 v[44:47], v[12:15], a[104:111], v116\n" \
"ds_read_b128 v[88:91], v115 offset:18432\n" \
"v_permlane32_swap_b32 v40, v42\n" \
"v_smfmac_f32_16x16x64_bf16 v[44:47], v[16:19], a[152:159], v116\n" \
"ds_read_b128 v[92:95], v115 offset:19456\n" \
"v_permlane32_swap_b32 v41, v43\n" \
"v_smfmac_f32_16x16x64_bf16 v[44:47], v[32:35], v[168:175], v116\n" \
"ds_read_b128 v[96:99], v115 offset:20480\n" \
"v_add_f32 v40, v40, v42\n" \
"v_smfmac_f32_16x16x64_bf16 v[44:47], v[36:39], v[216:223], v116\n" \
"ds_read_b128 v[100:103], v115 offset:21504\n" \
"v_add_f32 v41, v41, v43\n" \
"v_smfmac_f32_16x16x64_bf16 v[48:51], v[0:3], a[16:23], v116\n" \
"ds_read_b128 v[104:107], v115 offset:22528\n" \
"v_cvt_pk_bf16_f32 v122, v40, v41\n" \
"v_smfmac_f32_16x16x64_bf16 v[48:51], v[4:7], a[64:71], v116\n" \
"ds_write_b32 v117, v122 offset:0\n" \
"v_smfmac_f32_16x16x64_bf16 v[48:51], v[12:15], a[112:119], v116\n" \
"ds_read_b128 v[108:111], v115 offset:23552\n" \
"v_permlane32_swap_b32 v44, v46\n" \
"v_smfmac_f32_16x16x64_bf16 v[48:51], v[16:19], a[160:167], v116\n" \
"v_permlane32_swap_b32 v45, v47\n" \
"v_smfmac_f32_16x16x64_bf16 v[48:51], v[32:35], v[176:183], v116\n" \
"v_add_f32 v44, v44, v46\n" \
"v_smfmac_f32_16x16x64_bf16 v[48:51], v[36:39], v[224:231], v116\n" \
"v_add_f32 v45, v45, v47\n" \
"s_waitcnt lgkmcnt(11)\n" \
"v_smfmac_f32_16x16x64_bf16 v[52:55], v[8:11], v[64:71], v116\n" \
"v_cvt_pk_bf16_f32 v123, v44, v45\n" \
"s_waitcnt lgkmcnt(9)\n" \
"v_smfmac_f32_16x16x64_bf16 v[56:59], v[8:11], v[72:79], v116\n" \
"ds_write_b32 v117, v123 offset:512\n" \
"s_waitcnt lgkmcnt(8)\n" \
"v_smfmac_f32_16x16x64_bf16 v[60:63], v[8:11], v[80:87], v116\n" \
"ds_read_b128 v[64:67], v127 offset:6144\n" \
"v_permlane32_swap_b32 v48, v50\n" \
"s_waitcnt lgkmcnt(7)\n" \
"v_smfmac_f32_16x16x64_bf16 v[52:55], v[28:31], v[88:95], v116\n" \
"ds_read_b64 v[68:69], v127 offset:1032\n" \
"v_permlane32_swap_b32 v49, v51\n" \
"s_waitcnt lgkmcnt(6)\n" \
"v_smfmac_f32_16x16x64_bf16 v[56:59], v[28:31], v[96:103], v116\n" \
"ds_read_b64 v[70:71], v127 offset:7176\n" \
"v_add_f32 v48, v48, v50\n" \
"s_waitcnt lgkmcnt(4)\n" \
"v_smfmac_f32_16x16x64_bf16 v[60:63], v[28:31], v[104:111], v116\n" \
"ds_read_b128 v[72:75], v127 offset:8192\n" \
"v_add_f32 v49, v49, v51\n" \
"v_smfmac_f32_16x16x64_bf16 v[52:55], v[0:3], a[24:31], v116\n" \
"ds_read_b128 v[76:79], v127 offset:9216\n" \
"v_cvt_pk_bf16_f32 v122, v48, v49\n" \
"v_smfmac_f32_16x16x64_bf16 v[52:55], v[4:7], a[72:79], v116\n" \
"ds_write_b32 v117, v122 offset:1024\n" \
"v_smfmac_f32_16x16x64_bf16 v[52:55], v[12:15], a[120:127], v116\n" \
"ds_read_b64 v[80:81], v127 offset:4104\n" \
"s_waitcnt lgkmcnt(1)\n" \
"s_barrier\n" \
"v_smfmac_f32_16x16x64_bf16 v[52:55], v[16:19], a[168:175], v116\n" \
"ds_read_b64 v[82:83], v127 offset:10248\n" \
"ds_read_b64_tr_b16 v[88:89], v118 offset:0\n" \
"ds_read_b64_tr_b16 v[90:91], v118 offset:512\n" \
"v_smfmac_f32_16x16x64_bf16 v[52:55], v[32:35], v[184:191], v116\n" \
"ds_read_b128 v[84:87], v127 offset:11264\n" \
"ds_read_b64_tr_b16 v[92:93], v118 offset:1024\n" \
"ds_read_b64_tr_b16 v[94:95], v118 offset:1536\n" \
"v_smfmac_f32_16x16x64_bf16 v[52:55], v[36:39], v[232:239], v116\n" \
"ds_read_b64_tr_b16 v[96:97], v118 offset:2048\n" \
"ds_read_b64_tr_b16 v[98:99], v118 offset:2560\n" \
"v_smfmac_f32_16x16x64_bf16 v[56:59], v[0:3], a[32:39], v116\n" \
"ds_read_b64_tr_b16 v[100:101], v118 offset:3072\n" \
"ds_read_b64_tr_b16 v[102:103], v118 offset:3584\n" \
"v_smfmac_f32_16x16x64_bf16 v[56:59], v[4:7], a[80:87], v116\n" \
"ds_read_b64_tr_b16 v[104:105], v118 offset:4096\n" \
"ds_read_b64_tr_b16 v[106:107], v118 offset:4608\n" \
"v_smfmac_f32_16x16x64_bf16 v[56:59], v[12:15], a[128:135], v116\n" \
"ds_read_b64_tr_b16 v[108:109], v118 offset:5120\n" \
"ds_read_b64_tr_b16 v[110:111], v118 offset:5632\n" \
"v_permlane32_swap_b32 v52, v54\n" \
"v_smfmac_f32_16x16x64_bf16 v[56:59], v[16:19], a[176:183], v116\n" \
"v_permlane32_swap_b32 v53, v55\n" \
"v_smfmac_f32_16x16x64_bf16 v[56:59], v[32:35], v[192:199], v116\n" \
"v_add_f32 v52, v52, v54\n" \
"v_smfmac_f32_16x16x64_bf16 v[56:59], v[36:39], v[240:247], v116\n" \
"v_add_f32 v53, v53, v55\n" \
"s_waitcnt lgkmcnt(11)\n" \
"v_mfma_f32_16x16x32_bf16 v[40:43], v[64:67], v[88:91], 0\n" \
"v_cvt_pk_bf16_f32 v123, v52, v53\n" \
"s_waitcnt lgkmcnt(8)\n" \
"v_mfma_f32_16x16x32_bf16 v[40:43], v[68:71], v[92:95], v[40:43]\n" \
"ds_write_b32 v117, v123 offset:16\n" \
"s_waitcnt lgkmcnt(7)\n" \
"v_mfma_f32_16x16x32_bf16 v[40:43], v[72:75], v[96:99], v[40:43]\n" \
"v_permlane32_swap_b32 v56, v58\n" \
"v_permlane32_swap_b32 v57, v59\n" \
"s_waitcnt lgkmcnt(5)\n" \
"v_mfma_f32_16x16x32_bf16 v[40:43], v[76:79], v[100:103], v[40:43]\n" \
"v_add_f32 v56, v56, v58\n" \
"v_add_f32 v57, v57, v59\n" \
"s_waitcnt lgkmcnt(3)\n" \
"v_mfma_f32_16x16x32_bf16 v[40:43], v[80:83], v[104:107], v[40:43]\n" \
"v_cvt_pk_bf16_f32 v122, v56, v57\n" \
"s_waitcnt lgkmcnt(1)\n" \
"v_mfma_f32_16x16x32_bf16 v[40:43], v[84:87], v[108:111], v[40:43]\n" \
"ds_write_b32 v117, v122 offset:528\n" \
"v_smfmac_f32_16x16x64_bf16 v[60:63], v[0:3], a[40:47], v116\n" \
"v_smfmac_f32_16x16x64_bf16 v[60:63], v[4:7], a[88:95], v116\n" \
"v_smfmac_f32_16x16x64_bf16 v[60:63], v[12:15], a[136:143], v116\n" \
"v_max3_f32 v44, v40, v41, v42\n" \
"v_smfmac_f32_16x16x64_bf16 v[60:63], v[16:19], a[184:191], v116\n" \
"v_max_f32 v44, v44, v43\n" \
"v_smfmac_f32_16x16x64_bf16 v[60:63], v[32:35], v[200:207], v116\n" \
"v_mov_b32 v45, v44\n" \
"v_smfmac_f32_16x16x64_bf16 v[60:63], v[36:39], v[248:255], v116\n" \
"s_nop 0\n" \
"v_permlane16_swap_b32 v44, v45\n" \
"v_max_f32 v44, v44, v45\n" \
"v_mov_b32 v45, v44\n" \
"s_nop 3\n" \
"v_permlane32_swap_b32 v60, v62\n" \
"v_permlane32_swap_b32 v61, v63\n" \
"v_add_f32 v60, v60, v62\n" \
"v_add_f32 v61, v61, v63\n" \
"v_cvt_pk_bf16_f32 v123, v60, v61\n" \
"ds_write_b32 v117, v123 offset:1040\n" \
"s_waitcnt lgkmcnt(0)\n" \
"s_barrier\n" \
"v_permlane32_swap_b32 v44, v45\n" \
"v_max_f32 v44, v44, v45\n" \
"v_mul_f32 v46, s30, v44\n" \
"v_fma_f32 v40, v40, s30, -v46\n" \
"v_fma_f32 v41, v41, s30, -v46\n" \
"v_fma_f32 v42, v42, s30, -v46\n" \
"v_fma_f32 v43, v43, s30, -v46\n" \
"v_exp_f32 v40, v40\n" \
"v_exp_f32 v41, v41\n" \
"v_exp_f32 v42, v42\n" \
"v_exp_f32 v43, v43\n" \
"v_add_f32 v45, v40, v41\n" \
"v_add_f32 v46, v42, v43\n" \
"v_add_f32 v45, v45, v46\n" \
"ds_write_b128 v114, v[40:43] offset:41984\n" \
"ds_write_b64 v126, v[44:45] offset:43520\n" \
"s_waitcnt lgkmcnt(0)\n" \
"s_barrier\n" \
"s_add_u32 s34, s21, 4096\n" \
"v_lshrrev_b32 v60, 7, v126\n" \
"v_and_b32 v61, 0x7f, v126\n" \
"v_lshl_add_u32 v60, v60, 14, v61\n" \
"buffer_load_dwordx2 v[122:123], v60, s[8:11], s34 offen offset:3584 sc1\n" \
"s_add_u32 s35, s26, 4096\n" \
"buffer_load_dwordx4 v[0:3], v114, s[8:11], s35 offen offset:2048 sc1\n" \
"buffer_load_dwordx2 v[4:5], v114, s[8:11], s26 offen offset:1032 sc1\n" \
"buffer_load_dwordx2 v[6:7], v114, s[8:11], s35 offen offset:3080 sc1\n" \
"s_add_u32 s35, s26, 8192\n" \
"buffer_load_dwordx4 v[8:11], v114, s[8:11], s35 offen offset:0 sc1\n" \
"buffer_load_dwordx4 v[12:15], v114, s[8:11], s35 offen offset:1024 sc1\n" \
"s_add_u32 s35, s26, 4096\n" \
"buffer_load_dwordx2 v[16:17], v114, s[8:11], s35 offen offset:8 sc1\n" \
"s_add_u32 s35, s26, 8192\n" \
"buffer_load_dwordx2 v[18:19], v114, s[8:11], s35 offen offset:2056 sc1\n" \
"buffer_load_dwordx4 v[20:23], v114, s[8:11], s35 offen offset:3072 sc1\n" \
"s_add_u32 s26, s26, 12288\n" \
"s_sleep 2\n" \
"ds_read_b128 v[24:27], v127 offset:0\n" \
"ds_read_b64 v[28:29], v127 offset:1024\n" \
"ds_read_b64 v[30:31], v127 offset:7168\n" \
"ds_read_b128 v[32:35], v127 offset:2048\n" \
"ds_read_b128 v[36:39], v127 offset:3072\n" \
"ds_read_b64 v[40:41], v127 offset:4096\n" \
"ds_read_b64 v[42:43], v127 offset:10240\n" \
"ds_read_b128 v[44:47], v127 offset:5120\n" \
"ds_read_b64_tr_b16 v[88:89], v118 offset:0\n" \
"ds_read_b64_tr_b16 v[90:91], v118 offset:512\n" \
"ds_read_b64_tr_b16 v[92:93], v118 offset:1024\n" \
"ds_read_b64_tr_b16 v[94:95], v118 offset:1536\n" \
"ds_read_b64_tr_b16 v[96:97], v118 offset:2048\n" \
"ds_read_b64_tr_b16 v[98:99], v118 offset:2560\n" \
"ds_read_b64_tr_b16 v[100:101], v118 offset:3072\n" \
"ds_read_b64_tr_b16 v[102:103], v118 offset:3584\n" \
"ds_read_b64_tr_b16 v[104:105], v118 offset:4096\n" \
"ds_read_b64_tr_b16 v[106:107], v118 offset:4608\n" \
"ds_read_b64_tr_b16 v[108:109], v118 offset:5120\n" \
"ds_read_b64_tr_b16 v[110:111], v118 offset:5632\n" \
"v_mov_b32 v60, 0xff61b1e6\n" \
"s_waitcnt vmcnt(7)\n" \
"v_mfma_f32_16x16x32_bf16 v[48:51], v[64:67], v[0:3], 0\n" \
"s_waitcnt lgkmcnt(15)\n" \
"v_mfma_f32_16x16x32_bf16 v[52:55], v[24:27], v[0:3], 0\n" \
"s_waitcnt lgkmcnt(10)\n" \
"v_mfma_f32_16x16x32_bf16 v[56:59], v[88:91], v[0:3], 0\n" \
"s_waitcnt vmcnt(5)\n" \
"v_mfma_f32_16x16x32_bf16 v[48:51], v[68:71], v[4:7], v[48:51]\n" \
"v_mfma_f32_16x16x32_bf16 v[52:55], v[28:31], v[4:7], v[52:55]\n" \
"s_waitcnt lgkmcnt(8)\n" \
"v_mfma_f32_16x16x32_bf16 v[56:59], v[92:95], v[4:7], v[56:59]\n" \
"s_waitcnt vmcnt(4)\n" \
"v_mfma_f32_16x16x32_bf16 v[48:51], v[72:75], v[8:11], v[48:51]\n" \
"v_mfma_f32_16x16x32_bf16 v[52:55], v[32:35], v[8:11], v[52:55]\n" \
"s_waitcnt lgkmcnt(6)\n" \
"v_mfma_f32_16x16x32_bf16 v[56:59], v[96:99], v[8:11], v[56:59]\n" \
"s_waitcnt vmcnt(3)\n" \
"v_mfma_f32_16x16x32_bf16 v[48:51], v[76:79], v[12:15], v[48:51]\n" \
"v_mfma_f32_16x16x32_bf16 v[52:55], v[36:39], v[12:15], v[52:55]\n" \
"s_waitcnt lgkmcnt(4)\n" \
"v_mfma_f32_16x16x32_bf16 v[56:59], v[100:103], v[12:15], v[56:59]\n" \
"s_waitcnt vmcnt(1)\n" \
"v_mfma_f32_16x16x32_bf16 v[48:51], v[80:83], v[16:19], v[48:51]\n" \
"v_mfma_f32_16x16x32_bf16 v[52:55], v[40:43], v[16:19], v[52:55]\n" \
"s_waitcnt lgkmcnt(2)\n" \
"v_mfma_f32_16x16x32_bf16 v[56:59], v[104:107], v[16:19], v[56:59]\n" \
"s_waitcnt vmcnt(0)\n" \
"v_mfma_f32_16x16x32_bf16 v[48:51], v[84:87], v[20:23], v[48:51]\n" \
"v_mfma_f32_16x16x32_bf16 v[52:55], v[44:47], v[20:23], v[52:55]\n" \
"s_waitcnt lgkmcnt(0)\n" \
"v_mfma_f32_16x16x32_bf16 v[56:59], v[108:111], v[20:23], v[56:59]\n" \
"s_nop 4\n" \
"v_cndmask_b32 v52, v60, v52, s[64:65]\n" \
"v_cndmask_b32 v53, v60, v53, s[66:67]\n" \
"v_cndmask_b32 v54, v60, v54, s[68:69]\n" \
"v_cndmask_b32 v55, v60, v55, s[70:71]\n" \
"v_cndmask_b32 v48, v60, v48, s[72:73]\n" \
"v_cndmask_b32 v49, v60, v49, s[74:75]\n" \
"v_cndmask_b32 v50, v60, v50, s[76:77]\n" \
"v_cndmask_b32 v51, v60, v51, s[78:79]\n" \
"v_cndmask_b32 v56, v60, v56, s[16:17]\n" \
"v_cndmask_b32 v57, v60, v57, s[16:17]\n" \
"v_cndmask_b32 v58, v60, v58, s[16:17]\n" \
"v_cndmask_b32 v59, v60, v59, s[16:17]\n" \
"v_max3_f32 v61, v48, v49, v50\n" \
"v_max3_f32 v62, v51, v52, v53\n" \
"v_max3_f32 v61, v61, v54, v55\n" \
"v_max3_f32 v62, v62, v56, v57\n" \
"v_max3_f32 v61, v61, v58, v59\n" \
"v_max_f32 v61, v61, v62\n" \
"v_mov_b32 v62, v61\n" \
"s_nop 1\n" \
"v_permlane16_swap_b32 v61, v62\n" \
"v_max_f32 v61, v61, v62\n" \
"v_mov_b32 v62, v61\n" \
"s_nop 1\n" \
"v_permlane32_swap_b32 v61, v62\n" \
"v_max_f32 v61, v61, v62\n" \
"v_mul_f32 v61, s30, v61\n" \
"v_fma_f32 v48, v48, s30, -v61\n" \
"v_fma_f32 v49, v49, s30, -v61\n" \
"v_fma_f32 v50, v50, s30, -v61\n" \
"v_fma_f32 v51, v51, s30, -v61\n" \
"v_fma_f32 v52, v52, s30, -v61\n" \
"v_fma_f32 v53, v53, s30, -v61\n" \
"v_fma_f32 v54, v54, s30, -v61\n" \
"v_fma_f32 v55, v55, s30, -v61\n" \
"v_fma_f32 v56, v56, s30, -v61\n" \
"v_fma_f32 v57, v57, s30, -v61\n" \
"v_fma_f32 v58, v58, s30, -v61\n" \
"v_fma_f32 v59, v59, s30, -v61\n" \
"v_exp_f32 v48, v48\n" \
"v_exp_f32 v49, v49\n" \
"v_exp_f32 v50, v50\n" \
"v_exp_f32 v51, v51\n" \
"v_exp_f32 v52, v52\n" \
"v_exp_f32 v53, v53\n" \
"v_exp_f32 v54, v54\n" \
"v_exp_f32 v55, v55\n" \
"v_exp_f32 v56, v56\n" \
"v_exp_f32 v57, v57\n" \
"v_exp_f32 v58, v58\n" \
"v_exp_f32 v59, v59\n" \
"v_add_f32 v61, v48, v49\n" \
"v_add_f32 v62, v50, v51\n" \
"v_add_f32 v61, v61, v52\n" \
"v_add_f32 v62, v62, v53\n" \
"v_add_f32 v61, v61, v54\n" \
"v_add_f32 v62, v62, v55\n" \
"v_add_f32 v61, v61, v56\n" \
"v_add_f32 v62, v62, v57\n" \
"v_add_f32 v61, v61, v58\n" \
"v_add_f32 v62, v62, v59\n" \
"v_add_f32 v61, v61, v62\n" \
"v_mov_b32 v62, v61\n" \
"s_nop 1\n" \
"v_permlane16_swap_b32 v61, v62\n" \
"v_add_f32 v61, v61, v62\n" \
"v_mov_b32 v62, v61\n" \
"s_nop 1\n" \
"v_permlane32_swap_b32 v61, v62\n" \
"v_add_f32 v61, v61, v62\n" \
"v_rcp_f32 v61, v61\n" \
"s_nop 0\n" \
"v_mul_f32 v48, v48, v61\n" \
"v_mul_f32 v49, v49, v61\n" \
"v_mul_f32 v50, v50, v61\n" \
"v_mul_f32 v51, v51, v61\n" \
"v_mul_f32 v52, v52, v61\n" \
"v_mul_f32 v53, v53, v61\n" \
"v_mul_f32 v54, v54, v61\n" \
"v_mul_f32 v55, v55, v61\n" \
"v_mul_f32 v56, v56, v61\n" \
"v_mul_f32 v57, v57, v61\n" \
"v_mul_f32 v58, v58, v61\n" \
"v_mul_f32 v59, v59, v61\n" \
"v_cvt_pk_bf16_f32 v24, v52, v53\n" \
"v_cvt_pk_bf16_f32 v25, v54, v55\n" \
"v_cvt_pk_bf16_f32 v26, v48, v49\n" \
"v_cvt_pk_bf16_f32 v27, v50, v51\n" \
"v_cvt_pk_bf16_f32 v28, v56, v57\n" \
"v_cvt_pk_bf16_f32 v29, v58, v59\n" \
"buffer_store_dwordx4 v[24:27], v114, s[8:11], s25 offen sc1\n" \
"s_mov_b64 exec, s[16:17]\n" \
"buffer_store_dwordx2 v[28:29], v126, s[8:11], s25 offen offset:768 sc1\n" \
"s_mov_b64 exec, -1\n" \
"s_add_u32 s25, s25, 81920\n" \
"v_xor_b32 v127, 12288, v127\n" \
"s_cmp_eq_u32 s18, 3\n" \
"s_cbranch_scc1 LS2_dec\n" \
"LS2_dec_soft:\n" \
"v_max_i32 v122, v122, v123\n" \
"s_nop 1\n" \
"v_max_i32_dpp v122, v122, v122 row_shr:1 row_mask:0xf bank_mask:0xf\n" \
"s_nop 1\n" \
"v_max_i32_dpp v122, v122, v122 row_shr:2 row_mask:0xf bank_mask:0xf\n" \
"s_nop 1\n" \
"v_max_i32_dpp v122, v122, v122 row_shr:4 row_mask:0xf bank_mask:0xf\n" \
"s_nop 1\n" \
"v_max_i32_dpp v122, v122, v122 row_shr:8 row_mask:0xf bank_mask:0xf\n" \
"s_nop 0\n" \
"v_readlane_b32 s62, v122, 15\n" \
"v_readlane_b32 s63, v122, 31\n" \
"v_readlane_b32 s38, v122, 47\n" \
"v_readlane_b32 s39, v122, 63\n" \
"s_max_i32 s62, s62, s63\n" \
"s_max_i32 s38, s38, s39\n" \
"s_max_i32 s62, s62, s38\n" \
"s_add_u32 s62, s62, s31\n" \
"s_add_u32 s18, s18, 1\n" \
"s_cmp_eq_u32 s18, 40\n" \
"s_cselect_b32 s34, 4, 0\n" \
"s_sub_u32 s31, s31, s34\n" \
"s_cmp_eq_u32 s18, 64\n" \
"s_cbranch_scc1 LS_end\n" \
"s_cmp_eq_u32 s18, 32\n" \
"s_cselect_b32 s35, s14, 0\n" \
"s_cmp_eq_u32 s18, 5\n" \
"s_cselect_b32 s35, s15, s35\n" \
"s_add_u32 s35, s35, 65536\n" \
"s_add_u32 s21, s21, s35\n" \
"s_add_u32 s24, s24, s35\n" \
"s_add_u32 s27, s27, 2048\n" \
"s_branch LS2_loop\n" \
"LS2_qr:\n" \
"s_cmp_lg_u32 s19, 0\n" \
"s_cbranch_scc1 LS2_q_soft\n" \
"s_waitcnt lgkmcnt(0)\n" \
"s_mov_b64 exec, vcc\n" \
"buffer_load_dwordx4 v[20:23], v112, s[8:11], s21 offen sc1\n" \
"buffer_load_dwordx4 v[24:27], v112, s[8:11], s21 offen offset:1024 sc1\n" \
"buffer_load_dwordx2 v[28:29], v113, s[8:11], s21 offen sc1\n" \
"s_mov_b64 exec, -1\n" \
"s_sub_u32 s20, s20, 1\n" \
"s_cselect_b32 s19, 1, 0\n" \
"s_nop 7\n" \
"v_mov_b64 v[40:41], 0\n" \
"v_mov_b64 v[42:43], 0\n" \
"v_mov_b64 v[44:45], 0\n" \
"v_mov_b64 v[46:47], 0\n" \
"v_mov_b64 v[48:49], 0\n" \
"v_mov_b64 v[50:51], 0\n" \
"s_branch LS2_q\n" \
"LS2_dec:\n" \
"s_mov_b32 s20, 262144\n" \
"v_lshrrev_b32 v60, 7, v126\n" \
"v_and_b32 v61, 0x7f, v126\n" \
"v_lshl_add_u32 v60, v60, 14, v61\n" \
"LS2_dl:\n" \
"s_nop 1\n" \
"v_readlane_b32 s35, v122, 8\n" \
"s_cmp_lg_u32 s35, -1\n" \
"s_cbranch_scc1 LS2_dok\n" \
"s_cmp_lg_u32 s19, 0\n" \
"s_cbranch_scc1 LS2_dok\n" \
"s_sleep 4\n" \
"buffer_load_dwordx2 v[122:123], v60, s[8:11], s34 offen offset:3584 sc1\n" \
"s_sub_u32 s20, s20, 1\n" \
"s_cselect_b32 s19, 1, 0\n" \
"s_waitcnt vmcnt(0)\n" \
"s_branch LS2_dl\n" \
"LS2_dok:\n" \
"s_and_b32 s15, s35, 1\n" \
"s_lshl_b32 s15, s15, 13\n" \
"s_and_b32 s14, s35, 2\n" \
"s_lshl_b32 s14, s14, 12\n" \
"s_sub_u32 s14, s14, s15\n" \
"s_branch LS2_dec_soft\n" \
"LS3:\n" \
"s_mov_b32 s20, 262144\n" \
"LS3_fw0:\n" \
"buffer_load_dword v122, off, s[8:11], s27 offset:0 sc1\n" \
"buffer_load_dword v123, off, s[8:11], s27 offset:128 sc1\n" \
"s_sub_u32 s20, s20, 1\n" \
"s_cselect_b32 s19, 1, s19\n" \
"s_waitcnt vmcnt(0)\n" \
"v_min_u32 v122, v122, v123\n" \
"s_nop 0\n" \
"v_readfirstlane_b32 s34, v122\n" \
"s_or_b32 s34, s34, s19\n" \
"s_cbranch_scc0 LS3_fw0\n" \
"s_add_u32 m0, s29, 0\n" \
"s_nop 0\n" \
"buffer_load_dwordx4 v114, s[8:11], s28 offen offset:0 sc1 lds\n" \
"buffer_load_dwordx4 v114, s[8:11], s28 offen offset:1024 sc1 lds\n" \
"buffer_load_dwordx4 v114, s[8:11], s28 offen offset:2048 sc1 lds\n" \
"buffer_load_dwordx4 v114, s[8:11], s28 offen offset:3072 sc1 lds\n" \
"s_add_u32 m0, s29, 4096\n" \
"s_add_u32 s28, s28, 4096\n" \
"buffer_load_dwordx4 v114, s[8:11], s28 offen offset:0 sc1 lds\n" \
"buffer_load_dwordx4 v114, s[8:11], s28 offen offset:1024 sc1 lds\n" \
"buffer_load_dwordx4 v114, s[8:11], s28 offen offset:2048 sc1 lds\n" \
"buffer_load_dwordx4 v114, s[8:11], s28 offen offset:3072 sc1 lds\n" \
"s_add_u32 m0, s29, 8192\n" \
"s_add_u32 s28, s28, 4096\n" \
"buffer_load_dwordx4 v114, s[8:11], s28 offen offset:0 sc1 lds\n" \
"buffer_load_dwordx4 v114, s[8:11], s28 offen offset:1024 sc1 lds\n" \
"buffer_load_dwordx4 v114, s[8:11], s28 offen offset:2048 sc1 lds\n" \
"buffer_load_dwordx4 v114, s[8:11], s28 offen offset:3072 sc1 lds\n" \
"s_add_u32 s28, s28, 4096\n" \
"s_xor_b32 s29, s29, 12288\n" \
"s_branch LS3_go\n" \
".p2align 6\n" \
"LS3_loop:\n" \
"s_waitcnt lgkmcnt(0)\n" \
"LS3_tm:\n" \
"s_memrealtime s[38:39]\n" \
"s_waitcnt lgkmcnt(0)\n" \
"s_sub_u32 s38, s38, s62\n" \
"s_cmp_lt_i32 s38, 0\n" \
"s_cbranch_scc1 LS3_tm\n" \
"LS3_go:\n" \
"buffer_load_dwordx4 v[32:35], v112, s[8:11], s21 offen sc1\n" \
"buffer_load_dwordx4 v[36:39], v112, s[8:11], s21 offen offset:1024 sc1\n" \
"buffer_load_dwordx2 v[28:29], v113, s[8:11], s21 offen sc1\n" \
"ds_read_b128 v[64:67], v115 offset:0\n" \
"ds_read_b128 v[68:71], v115 offset:1024\n" \
"ds_read_b128 v[72:75], v115 offset:2048\n" \
"ds_read_b128 v[76:79], v115 offset:3072\n" \
"ds_read_b128 v[80:83], v115 offset:4096\n" \
"ds_read_b128 v[84:87], v115 offset:5120\n" \
"ds_read_b128 v[88:91], v115 offset:12288\n" \
"ds_read_b128 v[92:95], v115 offset:13312\n" \
"ds_read_b128 v[96:99], v115 offset:14336\n" \
"ds_read_b128 v[100:103], v115 offset:15360\n" \
"ds_read_b128 v[104:107], v115 offset:16384\n" \
"ds_read_b128 v[108:111], v115 offset:17408\n" \
"v_mov_b64 v[40:41], 0\n" \
"v_mov_b64 v[42:43], 0\n" \
"v_mov_b64 v[44:45], 0\n" \
"v_mov_b64 v[46:47], 0\n" \
"v_mov_b64 v[48:49], 0\n" \
"v_mov_b64 v[50:51], 0\n" \
"v_mov_b64 v[52:53], 0\n" \
"v_mov_b64 v[54:55], 0\n" \
"v_mov_b64 v[56:57], 0\n" \
"v_mov_b64 v[58:59], 0\n" \
"v_mov_b64 v[60:61], 0\n" \
"v_mov_b64 v[62:63], 0\n" \
"s_waitcnt lgkmcnt(0)\n" \
"s_mov_b32 s20, 262144\n" \
"LS3_q:\n" \
"s_waitcnt vmcnt(2)\n" \
"ds_write_b128 v124, v[32:35] offset:0\n" \
"v_smfmac_f32_16x16x64_bf16 v[40:43], v[32:35], v[160:167], v116\n" \
"s_waitcnt vmcnt(1)\n" \
"ds_write_b128 v124, v[36:39] offset:1024\n" \
"v_max3_u32 v122, v32, v33, v34\n" \
"v_max3_u32 v122, v122, v35, v36\n" \
"v_smfmac_f32_16x16x64_bf16 v[44:47], v[32:35], v[168:175], v116\n" \
"s_waitcnt vmcnt(0)\n" \
"ds_write_b64 v125, v[28:29] offset:0\n" \
"v_max3_u32 v122, v122, v37, v38\n" \
"v_max3_u32 v122, v122, v39, v28\n" \
"v_smfmac_f32_16x16x64_bf16 v[48:51], v[32:35], v[176:183], v116\n" \
"v_max_u32 v122, v122, v29\n" \
"v_cmp_eq_u32 vcc, -1, v122\n" \
"s_cbranch_vccnz LS3_qr\n" \
"LS3_q_soft:\n" \
"s_waitcnt lgkmcnt(0)\n" \
"s_barrier\n" \
"s_memrealtime s[60:61]\n" \
"v_smfmac_f32_16x16x64_bf16 v[40:43], v[36:39], v[208:215], v116\n" \
"ds_read_b128 v[8:11], v114 offset:2048\n" \
"v_smfmac_f32_16x16x64_bf16 v[44:47], v[36:39], v[216:223], v116\n" \
"ds_read_b128 v[28:31], v114 offset:7168\n" \
"v_smfmac_f32_16x16x64_bf16 v[48:51], v[36:39], v[224:231], v116\n" \
"ds_read_b128 v[0:3], v114 offset:0\n" \
"v_smfmac_f32_16x16x64_bf16 v[52:55], v[32:35], v[184:191], v116\n" \
"ds_read_b128 v[4:7], v114 offset:1024\n" \
"v_smfmac_f32_16x16x64_bf16 v[56:59], v[32:35], v[192:199], v116\n" \
"ds_read_b128 v[12:15], v114 offset:3072\n" \
"v_smfmac_f32_16x16x64_bf16 v[60:63], v[32:35], v[200:207], v116\n" \
"ds_read_b128 v[16:19], v114 offset:4096\n" \
"v_smfmac_f32_16x16x64_bf16 v[52:55], v[36:39], v[232:239], v116\n" \
"ds_read_b128 v[20:23], v114 offset:5120\n" \
"v_smfmac_f32_16x16x64_bf16 v[56:59], v[36:39], v[240:247], v116\n" \
"ds_read_b128 v[24:27], v114 offset:6144\n" \
"v_smfmac_f32_16x16x64_bf16 v[60:63], v[36:39], v[248:255], v116\n" \
"s_waitcnt lgkmcnt(7)\n" \
"v_smfmac_f32_16x16x64_bf16 v[40:43], v[8:11], v[64:71], v116\n" \
"v_smfmac_f32_16x16x64_bf16 v[44:47], v[8:11], v[72:79], v116\n" \
"s_waitcnt lgkmcnt(0)\n" \
"s_sub_u32 s59, s60, s3\n" \
"s_add_u32 s59, s59, s58\n" \
"v_mov_b32 v122, s59\n" \
"s_add_u32 s34, s21, s2\n" \
"s_mov_b64 exec, 1\n" \
"buffer_store_dword v122, off, s[8:11], s34 sc1\n" \
"s_cmp_lg_u32 s18, 0\n" \
"s_cselect_b64 exec, 1, 0\n" \
"buffer_store_dword v116, off, s[8:11], s37 sc1\n" \
"s_add_u32 s37, s37, 4096\n" \
"s_mov_b64 exec, -1\n" \
"v_smfmac_f32_16x16x64_bf16 v[48:51], v[8:11], v[80:87], v116\n" \
"v_smfmac_f32_16x16x64_bf16 v[40:43], v[28:31], v[88:95], v116\n" \
"v_smfmac_f32_16x16x64_bf16 v[44:47], v[28:31], v[96:103], v116\n" \
"v_smfmac_f32_16x16x64_bf16 v[48:51], v[28:31], v[104:111], v116\n" \
"v_smfmac_f32_16x16x64_bf16 v[40:43], v[0:3], a[0:7], v116\n" \
"v_smfmac_f32_16x16x64_bf16 v[40:43], v[4:7], a[48:55], v116\n" \
"v_smfmac_f32_16x16x64_bf16 v[40:43], v[12:15], a[96:103], v116\n" \
"ds_read_b128 v[64:67], v115 offset:6144\n" \
"v_smfmac_f32_16x16x64_bf16 v[40:43], v[16:19], a[144:151], v116\n" \
"ds_read_b128 v[68:71], v115 offset:7168\n" \
"v_smfmac_f32_16x16x64_bf16 v[40:43], v[20:23], a[192:199], v116\n" \
"ds_read_b128 v[72:75], v115 offset:8192\n" \
"v_smfmac_f32_16x16x64_bf16 v[40:43], v[24:27], a[240:247], v116\n" \
"ds_read_b128 v[76:79], v115 offset:9216\n" \
"v_smfmac_f32_16x16x64_bf16 v[44:47], v[0:3], a[8:15], v116\n" \
"ds_read_b128 v[80:83], v115 offset:10240\n" \
"v_smfmac_f32_16x16x64_bf16 v[44:47], v[4:7], a[56:63], v116\n" \
"ds_read_b128 v[84:87], v115 offset:11264\n" \
"v_smfmac_f32_16x16x64_bf16 v[44:47], v[12:15], a[104:111], v116\n" \
"ds_read_b128 v[88:91], v115 offset:18432\n" \
"v_permlane32_swap_b32 v40, v42\n" \
"v_smfmac_f32_16x16x64_bf16 v[44:47], v[16:19], a[152:159], v116\n" \
"ds_read_b128 v[92:95], v115 offset:19456\n" \
"v_permlane32_swap_b32 v41, v43\n" \
"v_smfmac_f32_16x16x64_bf16 v[44:47], v[20:23], a[200:207], v116\n" \
"ds_read_b128 v[96:99], v115 offset:20480\n" \
"v_add_f32 v40, v40, v42\n" \
"v_smfmac_f32_16x16x64_bf16 v[44:47], v[24:27], a[248:255], v116\n" \
"ds_read_b128 v[100:103], v115 offset:21504\n" \
"v_add_f32 v41, v41, v43\n" \
"v_smfmac_f32_16x16x64_bf16 v[48:51], v[0:3], a[16:23], v116\n" \
"ds_read_b128 v[104:107], v115 offset:22528\n" \
"v_cvt_pk_bf16_f32 v122, v40, v41\n" \
"v_smfmac_f32_16x16x64_bf16 v[48:51], v[4:7], a[64:71], v116\n" \
"ds_write_b32 v117, v122 offset:0\n" \
"v_smfmac_f32_16x16x64_bf16 v[48:51], v[12:15], a[112:119], v116\n" \
"ds_read_b128 v[108:111], v115 offset:23552\n" \
"v_permlane32_swap_b32 v44, v46\n" \
"v_smfmac_f32_16x16x64_bf16 v[48:51], v[16:19], a[160:167], v116\n" \
"v_permlane32_swap_b32 v45, v47\n" \
"v_smfmac_f32_16x16x64_bf16 v[48:51], v[20:23], a[208:215], v116\n" \
"v_add_f32 v44, v44, v46\n" \
"v_smfmac_f32_16x16x64_bf16 v[48:51], v[24:27], v[128:135], v116\n" \
"v_add_f32 v45, v45, v47\n" \
"s_waitcnt lgkmcnt(11)\n" \
"v_smfmac_f32_16x16x64_bf16 v[52:55], v[8:11], v[64:71], v116\n" \
"v_cvt_pk_bf16_f32 v123, v44, v45\n" \
"s_waitcnt lgkmcnt(9)\n" \
"v_smfmac_f32_16x16x64_bf16 v[56:59], v[8:11], v[72:79], v116\n" \
"ds_write_b32 v117, v123 offset:512\n" \
"s_waitcnt lgkmcnt(8)\n" \
"v_smfmac_f32_16x16x64_bf16 v[60:63], v[8:11], v[80:87], v116\n" \
"v_permlane32_swap_b32 v48, v50\n" \
"s_waitcnt lgkmcnt(6)\n" \
"v_smfmac_f32_16x16x64_bf16 v[52:55], v[28:31], v[88:95], v116\n" \
"v_permlane32_swap_b32 v49, v51\n" \
"s_waitcnt lgkmcnt(4)\n" \
"v_smfmac_f32_16x16x64_bf16 v[56:59], v[28:31], v[96:103], v116\n" \
"v_add_f32 v48, v48, v50\n" \
"s_waitcnt lgkmcnt(1)\n" \
"v_smfmac_f32_16x16x64_bf16 v[60:63], v[28:31], v[104:111], v116\n" \
"v_add_f32 v49, v49, v51\n" \
"v_smfmac_f32_16x16x64_bf16 v[52:55], v[0:3], a[24:31], v116\n" \
"v_cvt_pk_bf16_f32 v122, v48, v49\n" \
"v_smfmac_f32_16x16x64_bf16 v[52:55], v[4:7], a[72:79], v116\n" \
"ds_write_b32 v117, v122 offset:1024\n" \
"v_smfmac_f32_16x16x64_bf16 v[52:55], v[12:15], a[120:127], v116\n" \
"s_waitcnt lgkmcnt(0)\n" \
"s_barrier\n" \
"v_smfmac_f32_16x16x64_bf16 v[52:55], v[16:19], a[168:175], v116\n" \
"v_smfmac_f32_16x16x64_bf16 v[52:55], v[20:23], a[216:223], v116\n" \
"v_smfmac_f32_16x16x64_bf16 v[52:55], v[24:27], v[136:143], v116\n" \
"v_smfmac_f32_16x16x64_bf16 v[56:59], v[0:3], a[32:39], v116\n" \
"v_smfmac_f32_16x16x64_bf16 v[56:59], v[4:7], a[80:87], v116\n" \
"v_smfmac_f32_16x16x64_bf16 v[56:59], v[12:15], a[128:135], v116\n" \
"v_permlane32_swap_b32 v52, v54\n" \
"v_smfmac_f32_16x16x64_bf16 v[56:59], v[16:19], a[176:183], v116\n" \
"v_permlane32_swap_b32 v53, v55\n" \
"v_smfmac_f32_16x16x64_bf16 v[56:59], v[20:23], a[224:231], v116\n" \
"v_add_f32 v52, v52, v54\n" \
"v_smfmac_f32_16x16x64_bf16 v[56:59], v[24:27], v[144:151], v116\n" \
"v_add_f32 v53, v53, v55\n" \
"v_smfmac_f32_16x16x64_bf16 v[60:63], v[0:3], a[40:47], v116\n" \
"v_cvt_pk_bf16_f32 v123, v52, v53\n" \
"v_smfmac_f32_16x16x64_bf16 v[60:63], v[4:7], a[88:95], v116\n" \
"ds_write_b32 v117, v123 offset:16\n" \
"v_smfmac_f32_16x16x64_bf16 v[60:63], v[12:15], a[136:143], v116\n" \
"v_permlane32_swap_b32 v56, v58\n" \
"v_smfmac_f32_16x16x64_bf16 v[60:63], v[16:19], a[184:191], v116\n" \
"v_permlane32_swap_b32 v57, v59\n" \
"v_smfmac_f32_16x16x64_bf16 v[60:63], v[20:23], a[232:239], v116\n" \
"v_add_f32 v56, v56, v58\n" \
"v_smfmac_f32_16x16x64_bf16 v[60:63], v[24:27], v[152:159], v116\n" \
"v_add_f32 v57, v57, v59\n" \
"v_cvt_pk_bf16_f32 v122, v56, v57\n" \
"ds_write_b32 v117, v122 offset:528\n" \
"s_nop 4\n" \
"v_permlane32_swap_b32 v60, v62\n" \
"v_permlane32_swap_b32 v61, v63\n" \
"v_add_f32 v60, v60, v62\n" \
"v_add_f32 v61, v61, v63\n" \
"v_cvt_pk_bf16_f32 v123, v60, v61\n" \
"ds_write_b32 v117, v123 offset:1040\n" \
"s_waitcnt lgkmcnt(0)\n" \
"s_barrier\n" \
"s_barrier\n" \
"s_memrealtime s[56:57]\n" \
"s_add_u32 s34, s21, 4096\n" \
"v_lshrrev_b32 v30, 7, v126\n" \
"v_and_b32 v31, 0x7f, v126\n" \
"v_lshl_add_u32 v30, v30, 14, v31\n" \
"buffer_load_dwordx2 v[32:33], v30, s[8:11], s34 offen offset:3584 sc1\n" \
"s_waitcnt lgkmcnt(0)\n" \
"s_sub_u32 s56, s56, s60\n" \
"s_min_u32 s58, s56, 150\n" \
"s_waitcnt vmcnt(0)\n" \
"s_cmp_eq_u32 s18, 3\n" \
"s_cbranch_scc1 LS3_dec\n" \
"LS3_dec_soft:\n" \
"v_max_i32 v32, v32, v33\n" \
"s_nop 1\n" \
"v_max_i32_dpp v32, v32, v32 row_shr:1 row_mask:0xf bank_mask:0xf\n" \
"s_nop 1\n" \
"v_max_i32_dpp v32, v32, v32 row_shr:2 row_mask:0xf bank_mask:0xf\n" \
"s_nop 1\n" \
"v_max_i32_dpp v32, v32, v32 row_shr:4 row_mask:0xf bank_mask:0xf\n" \
"s_nop 1\n" \
"v_max_i32_dpp v32, v32, v32 row_shr:8 row_mask:0xf bank_mask:0xf\n" \
"s_nop 0\n" \
"v_readlane_b32 s62, v32, 15\n" \
"v_readlane_b32 s63, v32, 31\n" \
"v_readlane_b32 s38, v32, 47\n" \
"v_readlane_b32 s39, v32, 63\n" \
"s_max_i32 s62, s62, s63\n" \
"s_max_i32 s38, s38, s39\n" \
"s_max_i32 s62, s62, s38\n" \
"s_add_u32 s62, s62, s31\n" \
"s_add_u32 s18, s18, 1\n" \
"s_cmp_eq_u32 s18, 40\n" \
"s_cselect_b32 s34, 4, 0\n" \
"s_sub_u32 s31, s31, s34\n" \
"s_cmp_eq_u32 s18, 64\n" \
"s_cbranch_scc1 LS_end\n" \
"s_mov_b32 s20, 262144\n" \
"LS3_fw:\n" \
"buffer_load_dword v122, off, s[8:11], s27 offset:2048 sc1\n" \
"buffer_load_dword v123, off, s[8:11], s27 offset:2176 sc1\n" \
"s_sub_u32 s20, s20, 1\n" \
"s_cselect_b32 s19, 1, s19\n" \
"s_waitcnt vmcnt(0)\n" \
"v_min_u32 v122, v122, v123\n" \
"s_nop 0\n" \
"v_readfirstlane_b32 s34, v122\n" \
"s_or_b32 s34, s34, s19\n" \
"s_cbranch_scc0 LS3_fw\n" \
"s_add_u32 m0, s29, 0\n" \
"s_nop 0\n" \
"buffer_load_dwordx4 v114, s[8:11], s28 offen offset:0 sc1 lds\n" \
"buffer_load_dwordx4 v114, s[8:11], s28 offen offset:1024 sc1 lds\n" \
"buffer_load_dwordx4 v114, s[8:11], s28 offen offset:2048 sc1 lds\n" \
"buffer_load_dwordx4 v114, s[8:11], s28 offen offset:3072 sc1 lds\n" \
"s_add_u32 m0, s29, 4096\n" \
"s_add_u32 s28, s28, 4096\n" \
"buffer_load_dwordx4 v114, s[8:11], s28 offen offset:0 sc1 lds\n" \
"buffer_load_dwordx4 v114, s[8:11], s28 offen offset:1024 sc1 lds\n" \
"buffer_load_dwordx4 v114, s[8:11], s28 offen offset:2048 sc1 lds\n" \
"buffer_load_dwordx4 v114, s[8:11], s28 offen offset:3072 sc1 lds\n" \
"s_add_u32 m0, s29, 8192\n" \
"s_add_u32 s28, s28, 4096\n" \
"buffer_load_dwordx4 v114, s[8:11], s28 offen offset:0 sc1 lds\n" \
"buffer_load_dwordx4 v114, s[8:11], s28 offen offset:1024 sc1 lds\n" \
"buffer_load_dwordx4 v114, s[8:11], s28 offen offset:2048 sc1 lds\n" \
"buffer_load_dwordx4 v114, s[8:11], s28 offen offset:3072 sc1 lds\n" \
"s_add_u32 s28, s28, 4096\n" \
"s_xor_b32 s29, s29, 12288\n" \
"s_cmp_eq_u32 s18, 32\n" \
"s_cselect_b32 s35, s14, 0\n" \
"s_cmp_eq_u32 s18, 5\n" \
"s_cselect_b32 s35, s15, s35\n" \
"s_add_u32 s35, s35, 65536\n" \
"s_add_u32 s21, s21, s35\n" \
"s_add_u32 s24, s24, s35\n" \
"s_add_u32 s27, s27, 2048\n" \
"s_branch LS3_loop\n" \
"LS3_qr:\n" \
"s_cmp_lg_u32 s19, 0\n" \
"s_cbranch_scc1 LS3_q_soft\n" \
"s_waitcnt lgkmcnt(0)\n" \
"s_mov_b64 exec, vcc\n" \
"buffer_load_dwordx4 v[32:35], v112, s[8:11], s21 offen sc1\n" \
"buffer_load_dwordx4 v[36:39], v112, s[8:11], s21 offen offset:1024 sc1\n" \
"buffer_load_dwordx2 v[28:29], v113, s[8:11], s21 offen sc1\n" \
"s_mov_b64 exec, -1\n" \
"s_sub_u32 s20, s20, 1\n" \
"s_cselect_b32 s19, 1, 0\n" \
"s_nop 7\n" \
"v_mov_b64 v[40:41], 0\n" \
"v_mov_b64 v[42:43], 0\n" \
"v_mov_b64 v[44:45], 0\n" \
"v_mov_b64 v[46:47], 0\n" \
"v_mov_b64 v[48:49], 0\n" \
"v_mov_b64 v[50:51], 0\n" \
"s_branch LS3_q\n" \
"LS3_dec:\n" \
"s_mov_b32 s20, 262144\n" \
"LS3_dl:\n" \
"s_nop 1\n" \
"v_readlane_b32 s35, v32, 8\n" \
"s_cmp_lg_u32 s35, -1\n" \
"s_cbranch_scc1 LS3_dok\n" \
"s_cmp_lg_u32 s19, 0\n" \
"s_cbranch_scc1 LS3_dok\n" \
"s_sleep 4\n" \
"buffer_load_dwordx2 v[32:33], v30, s[8:11], s34 offen offset:3584 sc1\n" \
"s_sub_u32 s20, s20, 1\n" \
"s_cselect_b32 s19, 1, 0\n" \
"s_waitcnt vmcnt(0)\n" \
"s_branch LS3_dl\n" \
"LS3_dok:\n" \
"s_and_b32 s15, s35, 1\n" \
"s_lshl_b32 s15, s15, 13\n" \
"s_and_b32 s14, s35, 2\n" \
"s_lshl_b32 s14, s14, 12\n" \
"s_sub_u32 s14, s14, s15\n" \
"s_branch LS3_dec_soft\n" \
"LS_end:\n" \
"s_waitcnt vmcnt(0)\n" \
"s_barrier\n" \
"s_cmp_eq_u32 s13, 3\n" \
"s_cselect_b64 exec, 1, 0\n" \
"buffer_store_dword v116, off, s[8:11], s37 sc1\n" \
"s_endpgm\n"
#define R_ASM \
"s_nop 4\n" \
"s_mov_b32 s18, 0\n" \
"s_mov_b32 s19, 0\n" \
"s_mov_b32 s21, s15\n" \
"s_mov_b32 s25, 109051904\n" \
"s_mov_b32 s26, 109056000\n" \
"s_lshl_b32 s27, s12, 7\n" \
"s_add_u32 s27, s27, 37224448\n" \
"s_lshl_b32 s29, s13, 10\n" \
"s_add_u32 s28, s29, 40960\n" \
"s_add_u32 s37, s29, 16384\n" \
"s_add_u32 s29, s29, 57344\n" \
"s_xor_b32 s37, s37, s29\n" \
"s_add_u32 s30, s15, 65536\n" \
"s_mov_b32 s32, 0x00ff00ff\n" \
"s_mov_b32 s33, 0xff00ff00\n" \
"s_mov_b32 s22, 0x55555555\n" \
"s_mov_b32 s23, s22\n" \
"v_bfe_u32 v166, v178, 5, 2\n" \
"v_lshl_add_u32 v167, v166, 2, v167\n" \
"v_mov_b32 v189, 1\n" \
"s_add_u32 s31, s3, 24\n" \
"s_mov_b32 s60, 0x000f000f\n" \
"s_mov_b32 s61, 0x000f000f\n" \
"ds_read_b128 v[0:3], v181 offset:0\n" \
"ds_read_b128 v[4:7], v181 offset:1024\n" \
"ds_read_b128 v[8:11], v181 offset:2048\n" \
"ds_read_b128 v[12:15], v181 offset:3072\n" \
"ds_read_b128 v[16:19], v181 offset:4096\n" \
"ds_read_b128 v[20:23], v181 offset:5120\n" \
"ds_read_b128 v[24:27], v181 offset:6144\n" \
"ds_read_b128 v[28:31], v181 offset:7168\n" \
"ds_read_b128 v[32:35], v181 offset:8192\n" \
"ds_read_b128 v[36:39], v181 offset:9216\n" \
"s_waitcnt lgkmcnt(0)\n" \
"v_mfma_f32_16x16x32_bf16 v[80:83], v[0:3], a[0:3], 0\n" \
"ds_read_b128 v[40:43], v181 offset:10240\n" \
"v_mfma_f32_16x16x32_bf16 v[84:87], v[0:3], a[80:83], 0\n" \
"ds_read_b128 v[44:47], v181 offset:11264\n" \
"v_mfma_f32_16x16x32_bf16 v[88:91], v[0:3], a[160:163], 0\n" \
"ds_read_b128 v[48:51], v181 offset:12288\n" \
"v_mfma_f32_16x16x32_bf16 v[92:95], v[0:3], a[240:243], 0\n" \
"ds_read_b128 v[52:55], v181 offset:13312\n" \
"v_mfma_f32_16x16x32_bf16 v[80:83], v[4:7], a[4:7], v[80:83]\n" \
"ds_read_b128 v[56:59], v181 offset:14336\n" \
"v_mfma_f32_16x16x32_bf16 v[84:87], v[4:7], a[84:87], v[84:87]\n" \
"ds_read_b128 v[60:63], v181 offset:15360\n" \
"v_mfma_f32_16x16x32_bf16 v[88:91], v[4:7], a[164:167], v[88:91]\n" \
"ds_read_b128 v[64:67], v181 offset:16384\n" \
"v_mfma_f32_16x16x32_bf16 v[92:95], v[4:7], a[244:247], v[92:95]\n" \
"ds_read_b128 v[68:71], v181 offset:17408\n" \
"v_mfma_f32_16x16x32_bf16 v[80:83], v[8:11], a[8:11], v[80:83]\n" \
"ds_read_b128 v[72:75], v181 offset:18432\n" \
"v_mfma_f32_16x16x32_bf16 v[84:87], v[8:11], a[88:91], v[84:87]\n" \
"ds_read_b128 v[76:79], v181 offset:19456\n" \
"v_mfma_f32_16x16x32_bf16 v[88:91], v[8:11], a[168:171], v[88:91]\n" \
"v_mfma_f32_16x16x32_bf16 v[92:95], v[8:11], a[248:251], v[92:95]\n" \
"v_mfma_f32_16x16x32_bf16 v[80:83], v[12:15], a[12:15], v[80:83]\n" \
"v_mfma_f32_16x16x32_bf16 v[84:87], v[12:15], a[92:95], v[84:87]\n" \
"v_mfma_f32_16x16x32_bf16 v[88:91], v[12:15], a[172:175], v[88:91]\n" \
"v_mfma_f32_16x16x32_bf16 v[92:95], v[12:15], a[252:255], v[92:95]\n" \
"v_mfma_f32_16x16x32_bf16 v[80:83], v[16:19], a[16:19], v[80:83]\n" \
"v_mfma_f32_16x16x32_bf16 v[84:87], v[16:19], a[96:99], v[84:87]\n" \
"v_mfma_f32_16x16x32_bf16 v[88:91], v[16:19], a[176:179], v[88:91]\n" \
"v_mfma_f32_16x16x32_bf16 v[92:95], v[16:19], v[192:195], v[92:95]\n" \
"v_mfma_f32_16x16x32_bf16 v[80:83], v[20:23], a[20:23], v[80:83]\n" \
"v_mfma_f32_16x16x32_bf16 v[84:87], v[20:23], a[100:103], v[84:87]\n" \
"v_mfma_f32_16x16x32_bf16 v[88:91], v[20:23], a[180:183], v[88:91]\n" \
"v_mfma_f32_16x16x32_bf16 v[92:95], v[20:23], v[196:199], v[92:95]\n" \
"v_mfma_f32_16x16x32_bf16 v[80:83], v[24:27], a[24:27], v[80:83]\n" \
"v_mfma_f32_16x16x32_bf16 v[84:87], v[24:27], a[104:107], v[84:87]\n" \
"v_mfma_f32_16x16x32_bf16 v[88:91], v[24:27], a[184:187], v[88:91]\n" \
"v_mfma_f32_16x16x32_bf16 v[92:95], v[24:27], v[200:203], v[92:95]\n" \
"v_mfma_f32_16x16x32_bf16 v[80:83], v[28:31], a[28:31], v[80:83]\n" \
"v_mfma_f32_16x16x32_bf16 v[84:87], v[28:31], a[108:111], v[84:87]\n" \
"v_mfma_f32_16x16x32_bf16 v[88:91], v[28:31], a[188:191], v[88:91]\n" \
"v_mfma_f32_16x16x32_bf16 v[92:95], v[28:31], v[204:207], v[92:95]\n" \
"s_waitcnt lgkmcnt(0)\n" \
".p2align 6\n" \
"s_nop 0\n" \
"s_nop 0\n" \
"s_nop 0\n" \
"s_nop 0\n" \
"s_nop 0\n" \
"s_nop 0\n" \
"s_nop 0\n" \
"LR_loop:\n" \
"v_mfma_f32_16x16x32_bf16 v[80:83], v[32:35], a[32:35], v[80:83]\n" \
"v_mfma_f32_16x16x32_bf16 v[84:87], v[32:35], a[112:115], v[84:87]\n" \
"v_mfma_f32_16x16x32_bf16 v[88:91], v[32:35], a[192:195], v[88:91]\n" \
"v_mov_b64 v[132:133], 0\n" \
"v_mfma_f32_16x16x32_bf16 v[92:95], v[32:35], v[208:211], v[92:95]\n" \
"s_add_u32 m0, s29, 0\n" \
"s_add_u32 s34, s28, 0\n" \
"v_mov_b64 v[134:135], 0\n" \
"v_mfma_f32_16x16x32_bf16 v[80:83], v[36:39], a[36:39], v[80:83]\n" \
"buffer_load_dwordx4 v178, s[8:11], s34 offen lds\n" \
"v_mov_b64 v[136:137], 0\n" \
"v_mfma_f32_16x16x32_bf16 v[84:87], v[36:39], a[116:119], v[84:87]\n" \
"v_mov_b64 v[138:139], 0\n" \
"v_mfma_f32_16x16x32_bf16 v[88:91], v[36:39], a[196:199], v[88:91]\n" \
"v_mov_b64 v[140:141], 0\n" \
"v_mfma_f32_16x16x32_bf16 v[92:95], v[36:39], v[212:215], v[92:95]\n" \
"v_mov_b64 v[142:143], 0\n" \
"v_mfma_f32_16x16x32_bf16 v[80:83], v[40:43], a[40:43], v[80:83]\n" \
"v_mov_b64 v[144:145], 0\n" \
"v_mfma_f32_16x16x32_bf16 v[84:87], v[40:43], a[120:123], v[84:87]\n" \
"v_mov_b64 v[146:147], 0\n" \
"v_mfma_f32_16x16x32_bf16 v[88:91], v[40:43], a[200:203], v[88:91]\n" \
"ds_read_b128 v[0:3], v181 offset:20480\n" \
"v_mov_b64 v[162:163], 0\n" \
"v_mfma_f32_16x16x32_bf16 v[92:95], v[40:43], v[216:219], v[92:95]\n" \
"s_add_u32 m0, s29, 4096\n" \
"s_add_u32 s34, s28, 4096\n" \
"v_mov_b64 v[164:165], 0\n" \
"v_mfma_f32_16x16x32_bf16 v[80:83], v[44:47], a[44:47], v[80:83]\n" \
"buffer_load_dwordx4 v178, s[8:11], s34 offen lds\n" \
"v_mfma_f32_16x16x32_bf16 v[84:87], v[44:47], a[124:127], v[84:87]\n" \
"ds_read_b128 v[4:7], v181 offset:21504\n" \
"v_mfma_f32_16x16x32_bf16 v[88:91], v[44:47], a[204:207], v[88:91]\n" \
"v_lshrrev_b32 v170, 8, v178\n" \
"v_bfe_u32 v171, v178, 1, 7\n" \
"v_mfma_f32_16x16x32_bf16 v[92:95], v[44:47], v[220:223], v[92:95]\n" \
"ds_read_b128 v[8:11], v181 offset:22528\n" \
"v_lshl_add_u32 v170, v170, 14, v171\n" \
"v_mfma_f32_16x16x32_bf16 v[80:83], v[48:51], a[48:51], v[80:83]\n" \
"v_mfma_f32_16x16x32_bf16 v[84:87], v[48:51], a[128:131], v[84:87]\n" \
"ds_read_b128 v[12:15], v181 offset:23552\n" \
"v_mfma_f32_16x16x32_bf16 v[88:91], v[48:51], a[208:211], v[88:91]\n" \
"v_mfma_f32_16x16x32_bf16 v[92:95], v[48:51], v[224:227], v[92:95]\n" \
"s_add_u32 m0, s29, 8192\n" \
"s_add_u32 s34, s28, 8192\n" \
"ds_read_b128 v[16:19], v181 offset:24576\n" \
"v_mfma_f32_16x16x32_bf16 v[80:83], v[52:55], a[52:55], v[80:83]\n" \
"buffer_load_dwordx4 v178, s[8:11], s34 offen lds\n" \
"v_mfma_f32_16x16x32_bf16 v[84:87], v[52:55], a[132:135], v[84:87]\n" \
"ds_read_b128 v[20:23], v181 offset:25600\n" \
"v_mfma_f32_16x16x32_bf16 v[88:91], v[52:55], a[212:215], v[88:91]\n" \
"v_mfma_f32_16x16x32_bf16 v[92:95], v[52:55], v[228:231], v[92:95]\n" \
"ds_read_b128 v[24:27], v181 offset:26624\n" \
"v_mfma_f32_16x16x32_bf16 v[80:83], v[56:59], a[56:59], v[80:83]\n" \
"v_mfma_f32_16x16x32_bf16 v[84:87], v[56:59], a[136:139], v[84:87]\n" \
"ds_read_b128 v[28:31], v181 offset:27648\n" \
"v_mfma_f32_16x16x32_bf16 v[88:91], v[56:59], a[216:219], v[88:91]\n" \
"v_mfma_f32_16x16x32_bf16 v[92:95], v[56:59], v[232:235], v[92:95]\n" \
"s_add_u32 m0, s29, 12288\n" \
"s_add_u32 s34, s28, 12288\n" \
"ds_read_b128 v[32:35], v181 offset:28672\n" \
"v_mfma_f32_16x16x32_bf16 v[80:83], v[60:63], a[60:63], v[80:83]\n" \
"buffer_load_dwordx4 v178, s[8:11], s34 offen lds\n" \
"v_mfma_f32_16x16x32_bf16 v[84:87], v[60:63], a[140:143], v[84:87]\n" \
"ds_read_b128 v[36:39], v181 offset:29696\n" \
"v_mfma_f32_16x16x32_bf16 v[88:91], v[60:63], a[220:223], v[88:91]\n" \
"v_mfma_f32_16x16x32_bf16 v[92:95], v[60:63], v[236:239], v[92:95]\n" \
"v_mfma_f32_16x16x32_bf16 v[80:83], v[64:67], a[64:67], v[80:83]\n" \
"v_mfma_f32_16x16x32_bf16 v[84:87], v[64:67], a[144:147], v[84:87]\n" \
"v_mfma_f32_16x16x32_bf16 v[88:91], v[64:67], a[224:227], v[88:91]\n" \
"v_mfma_f32_16x16x32_bf16 v[92:95], v[64:67], v[240:243], v[92:95]\n" \
"s_add_u32 m0, s29, 16384\n" \
"s_add_u32 s34, s28, 16384\n" \
"v_mfma_f32_16x16x32_bf16 v[80:83], v[68:71], a[68:71], v[80:83]\n" \
"buffer_load_dwordx4 v178, s[8:11], s34 offen lds\n" \
"v_mfma_f32_16x16x32_bf16 v[84:87], v[68:71], a[148:151], v[84:87]\n" \
"v_mfma_f32_16x16x32_bf16 v[88:91], v[68:71], a[228:231], v[88:91]\n" \
"v_mfma_f32_16x16x32_bf16 v[92:95], v[68:71], v[244:247], v[92:95]\n" \
"v_mfma_f32_16x16x32_bf16 v[80:83], v[72:75], a[72:75], v[80:83]\n" \
"v_mfma_f32_16x16x32_bf16 v[84:87], v[72:75], a[152:155], v[84:87]\n" \
"v_mfma_f32_16x16x32_bf16 v[88:91], v[72:75], a[232:235], v[88:91]\n" \
"v_mfma_f32_16x16x32_bf16 v[92:95], v[72:75], v[248:251], v[92:95]\n" \
"s_add_u32 m0, s29, 20480\n" \
"s_add_u32 s34, s28, 20480\n" \
"v_mfma_f32_16x16x32_bf16 v[80:83], v[76:79], a[76:79], v[80:83]\n" \
"buffer_load_dwordx4 v178, s[8:11], s34 offen lds\n" \
"v_mfma_f32_16x16x32_bf16 v[84:87], v[76:79], a[156:159], v[84:87]\n" \
"v_mfma_f32_16x16x32_bf16 v[88:91], v[76:79], a[236:239], v[88:91]\n" \
"v_mfma_f32_16x16x32_bf16 v[92:95], v[76:79], v[252:255], v[92:95]\n" \
"s_waitcnt lgkmcnt(9)\n" \
"v_mfma_f32_16x16x32_bf16 v[96:99], v[0:3], a[0:3], 0\n" \
"v_mfma_f32_16x16x32_bf16 v[100:103], v[0:3], a[80:83], 0\n" \
"v_mfma_f32_16x16x32_bf16 v[104:107], v[0:3], a[160:163], 0\n" \
"ds_read_b128 v[40:43], v181 offset:30720\n" \
"v_mfma_f32_16x16x32_bf16 v[108:111], v[0:3], a[240:243], 0\n" \
"s_add_u32 m0, s29, 24576\n" \
"s_add_u32 s34, s28, 24576\n" \
"s_waitcnt lgkmcnt(9)\n" \
"v_mfma_f32_16x16x32_bf16 v[96:99], v[4:7], a[4:7], v[96:99]\n" \
"buffer_load_dwordx4 v178, s[8:11], s34 offen lds\n" \
"v_mfma_f32_16x16x32_bf16 v[100:103], v[4:7], a[84:87], v[100:103]\n" \
"ds_read_b128 v[44:47], v181 offset:31744\n" \
"v_mfma_f32_16x16x32_bf16 v[104:107], v[4:7], a[164:167], v[104:107]\n" \
"v_mfma_f32_16x16x32_bf16 v[108:111], v[4:7], a[244:247], v[108:111]\n" \
"ds_read_b128 v[48:51], v181 offset:32768\n" \
"s_waitcnt lgkmcnt(10)\n" \
"v_mfma_f32_16x16x32_bf16 v[96:99], v[8:11], a[8:11], v[96:99]\n" \
"v_mfma_f32_16x16x32_bf16 v[100:103], v[8:11], a[88:91], v[100:103]\n" \
"ds_read_b128 v[52:55], v181 offset:33792\n" \
"v_mfma_f32_16x16x32_bf16 v[104:107], v[8:11], a[168:171], v[104:107]\n" \
"buffer_load_dwordx4 v[152:155], v176, s[8:11], s21 offen sc1\n" \
"v_mfma_f32_16x16x32_bf16 v[108:111], v[8:11], a[248:251], v[108:111]\n" \
"buffer_load_dwordx4 v[156:159], v176, s[8:11], s21 offen offset:1024 sc1\n" \
"s_waitcnt lgkmcnt(10)\n" \
"v_mfma_f32_16x16x32_bf16 v[96:99], v[12:15], a[12:15], v[96:99]\n" \
"buffer_load_dwordx2 v[160:161], v177, s[8:11], s21 offen sc1\n" \
"v_mfma_f32_16x16x32_bf16 v[100:103], v[12:15], a[92:95], v[100:103]\n" \
"ds_read_b128 v[56:59], v181 offset:34816\n" \
"v_mfma_f32_16x16x32_bf16 v[104:107], v[12:15], a[172:175], v[104:107]\n" \
"v_mfma_f32_16x16x32_bf16 v[108:111], v[12:15], a[252:255], v[108:111]\n" \
"s_add_u32 m0, s29, 28672\n" \
"s_add_u32 s34, s28, 28672\n" \
"ds_read_b128 v[60:63], v181 offset:35840\n" \
"s_waitcnt lgkmcnt(11)\n" \
"v_mfma_f32_16x16x32_bf16 v[96:99], v[16:19], a[16:19], v[96:99]\n" \
"buffer_load_dwordx4 v178, s[8:11], s34 offen lds\n" \
"v_mfma_f32_16x16x32_bf16 v[100:103], v[16:19], a[96:99], v[100:103]\n" \
"ds_read_b128 v[64:67], v181 offset:36864\n" \
"v_mfma_f32_16x16x32_bf16 v[104:107], v[16:19], a[176:179], v[104:107]\n" \
"v_mfma_f32_16x16x32_bf16 v[108:111], v[16:19], v[192:195], v[108:111]\n" \
"ds_read_b128 v[68:71], v181 offset:37888\n" \
"s_waitcnt lgkmcnt(12)\n" \
"v_mfma_f32_16x16x32_bf16 v[96:99], v[20:23], a[20:23], v[96:99]\n" \
"v_mfma_f32_16x16x32_bf16 v[100:103], v[20:23], a[100:103], v[100:103]\n" \
"ds_read_b128 v[72:75], v181 offset:38912\n" \
"v_mfma_f32_16x16x32_bf16 v[104:107], v[20:23], a[180:183], v[104:107]\n" \
"buffer_store_dwordx4 v[112:115], v185, s[8:11], s25 offen offset:0 sc1\n" \
"v_mfma_f32_16x16x32_bf16 v[108:111], v[20:23], v[196:199], v[108:111]\n" \
"ds_read_b128 v[76:79], v181 offset:39936\n" \
"s_waitcnt lgkmcnt(13)\n" \
"v_mfma_f32_16x16x32_bf16 v[96:99], v[24:27], a[24:27], v[96:99]\n" \
"v_mfma_f32_16x16x32_bf16 v[100:103], v[24:27], a[104:107], v[100:103]\n" \
"v_mfma_f32_16x16x32_bf16 v[104:107], v[24:27], a[184:187], v[104:107]\n" \
"v_mfma_f32_16x16x32_bf16 v[108:111], v[24:27], v[200:203], v[108:111]\n" \
"s_add_u32 m0, s29, 32768\n" \
"s_add_u32 s34, s28, 32768\n" \
"s_waitcnt lgkmcnt(12)\n" \
"v_mfma_f32_16x16x32_bf16 v[96:99], v[28:31], a[28:31], v[96:99]\n" \
"buffer_load_dwordx4 v178, s[8:11], s34 offen lds\n" \
"v_mfma_f32_16x16x32_bf16 v[100:103], v[28:31], a[108:111], v[100:103]\n" \
"v_mfma_f32_16x16x32_bf16 v[104:107], v[28:31], a[188:191], v[104:107]\n" \
"v_mfma_f32_16x16x32_bf16 v[108:111], v[28:31], v[204:207], v[108:111]\n" \
"s_waitcnt lgkmcnt(11)\n" \
"v_mfma_f32_16x16x32_bf16 v[96:99], v[32:35], a[32:35], v[96:99]\n" \
"v_mfma_f32_16x16x32_bf16 v[100:103], v[32:35], a[112:115], v[100:103]\n" \
"v_mfma_f32_16x16x32_bf16 v[104:107], v[32:35], a[192:195], v[104:107]\n" \
"buffer_store_dwordx4 v[116:119], v185, s[8:11], s25 offen offset:1024 sc1\n" \
"v_mfma_f32_16x16x32_bf16 v[108:111], v[32:35], v[208:211], v[108:111]\n" \
"s_waitcnt lgkmcnt(10)\n" \
"v_mfma_f32_16x16x32_bf16 v[96:99], v[36:39], a[36:39], v[96:99]\n" \
"v_mfma_f32_16x16x32_bf16 v[100:103], v[36:39], a[116:119], v[100:103]\n" \
"v_mfma_f32_16x16x32_bf16 v[104:107], v[36:39], a[196:199], v[104:107]\n" \
"v_mfma_f32_16x16x32_bf16 v[108:111], v[36:39], v[212:215], v[108:111]\n" \
"s_add_u32 m0, s29, 36864\n" \
"s_add_u32 s34, s28, 36864\n" \
"s_waitcnt lgkmcnt(9)\n" \
"v_mfma_f32_16x16x32_bf16 v[96:99], v[40:43], a[40:43], v[96:99]\n" \
"buffer_load_dwordx4 v178, s[8:11], s34 offen lds\n" \
"v_mfma_f32_16x16x32_bf16 v[100:103], v[40:43], a[120:123], v[100:103]\n" \
"v_mfma_f32_16x16x32_bf16 v[104:107], v[40:43], a[200:203], v[104:107]\n" \
"v_mfma_f32_16x16x32_bf16 v[108:111], v[40:43], v[216:219], v[108:111]\n" \
"s_waitcnt lgkmcnt(8)\n" \
"v_mfma_f32_16x16x32_bf16 v[96:99], v[44:47], a[44:47], v[96:99]\n" \
"v_mfma_f32_16x16x32_bf16 v[100:103], v[44:47], a[124:127], v[100:103]\n" \
"v_mfma_f32_16x16x32_bf16 v[104:107], v[44:47], a[204:207], v[104:107]\n" \
"buffer_store_dwordx4 v[120:123], v185, s[8:11], s25 offen offset:2048 sc1\n" \
"v_mfma_f32_16x16x32_bf16 v[108:111], v[44:47], v[220:223], v[108:111]\n" \
"s_waitcnt lgkmcnt(7)\n" \
"v_mfma_f32_16x16x32_bf16 v[96:99], v[48:51], a[48:51], v[96:99]\n" \
"v_mfma_f32_16x16x32_bf16 v[100:103], v[48:51], a[128:131], v[100:103]\n" \
"v_mfma_f32_16x16x32_bf16 v[104:107], v[48:51], a[208:211], v[104:107]\n" \
"v_mfma_f32_16x16x32_bf16 v[108:111], v[48:51], v[224:227], v[108:111]\n" \
"s_waitcnt lgkmcnt(6)\n" \
"v_mfma_f32_16x16x32_bf16 v[96:99], v[52:55], a[52:55], v[96:99]\n" \
"v_mfma_f32_16x16x32_bf16 v[100:103], v[52:55], a[132:135], v[100:103]\n" \
"v_mfma_f32_16x16x32_bf16 v[104:107], v[52:55], a[212:215], v[104:107]\n" \
"s_waitcnt vmcnt(6)\n" \
"ds_write_b128 v190, v[152:155] offset:0\n" \
"v_max3_u32 v166, v152, v153, v154\n" \
"v_mfma_f32_16x16x32_bf16 v[108:111], v[52:55], v[228:231], v[108:111]\n" \
"ds_write_b128 v190, v[156:159] offset:1024\n" \
"v_max3_u32 v166, v166, v155, v156\n" \
"v_max3_u32 v166, v166, v157, v158\n" \
"s_waitcnt lgkmcnt(7)\n" \
"v_mfma_f32_16x16x32_bf16 v[96:99], v[56:59], a[56:59], v[96:99]\n" \
"ds_write_b64 v191, v[160:161] offset:0\n" \
"v_max3_u32 v166, v166, v159, v160\n" \
"v_max_u32 v166, v166, v161\n" \
"v_mfma_f32_16x16x32_bf16 v[100:103], v[56:59], a[136:139], v[100:103]\n" \
"v_cmp_eq_u32 vcc, -1, v166\n" \
"v_mfma_f32_16x16x32_bf16 v[104:107], v[56:59], a[216:219], v[104:107]\n" \
"v_mfma_f32_16x16x32_bf16 v[108:111], v[56:59], v[232:235], v[108:111]\n" \
"s_cbranch_vccnz LR_mslow\n" \
"LR_m_soft:\n" \
"s_waitcnt lgkmcnt(0)\n" \
"s_barrier\n" \
"v_mfma_f32_16x16x32_bf16 v[96:99], v[60:63], a[60:63], v[96:99]\n" \
"ds_read_b128 v[0:3], v178 offset:0\n" \
"v_mfma_f32_16x16x32_bf16 v[100:103], v[60:63], a[140:143], v[100:103]\n" \
"ds_read_b128 v[4:7], v178 offset:1024\n" \
"v_mfma_f32_16x16x32_bf16 v[104:107], v[60:63], a[220:223], v[104:107]\n" \
"buffer_store_dwordx4 v[124:127], v185, s[8:11], s25 offen offset:3072 sc1\n" \
"v_mfma_f32_16x16x32_bf16 v[108:111], v[60:63], v[236:239], v[108:111]\n" \
"ds_read_b128 v[8:11], v178 offset:2048\n" \
"v_mfma_f32_16x16x32_bf16 v[96:99], v[64:67], a[64:67], v[96:99]\n" \
"ds_read_b128 v[12:15], v178 offset:3072\n" \
"v_mfma_f32_16x16x32_bf16 v[100:103], v[64:67], a[144:147], v[100:103]\n" \
"ds_read_b128 v[16:19], v178 offset:4096\n" \
"v_mfma_f32_16x16x32_bf16 v[104:107], v[64:67], a[224:227], v[104:107]\n" \
"ds_read_b128 v[20:23], v178 offset:5120\n" \
"v_xor_b32 v181, 0xa000, v181\n" \
"v_mfma_f32_16x16x32_bf16 v[108:111], v[64:67], v[240:243], v[108:111]\n" \
"ds_read_b128 v[24:27], v178 offset:6144\n" \
"s_waitcnt lgkmcnt(6)\n" \
"v_smfmac_f32_16x16x64_bf16 v[132:135], v[0:3], a[0:7], v182\n" \
"buffer_store_dwordx4 v[128:131], v185, s[8:11], s26 offen sc1\n" \
"v_smfmac_f32_16x16x64_bf16 v[136:139], v[0:3], a[80:87], v182\n" \
"s_add_u32 s25, s25, 20480\n" \
"ds_read_b128 v[28:31], v178 offset:7168\n" \
"v_cvt_pk_bf16_f32 v112, v80, v81\n" \
"v_smfmac_f32_16x16x64_bf16 v[140:143], v[0:3], a[160:167], v182\n" \
"s_add_u32 s26, s26, 20480\n" \
"ds_read_b128 v[32:35], v178 offset:8192\n" \
"v_cvt_pk_bf16_f32 v113, v82, v83\n" \
"v_smfmac_f32_16x16x64_bf16 v[144:147], v[0:3], a[240:247], v182\n" \
"s_add_u32 s28, s28, 40960\n" \
"ds_read_b128 v[36:39], v178 offset:9216\n" \
"v_cvt_pk_bf16_f32 v116, v84, v85\n" \
"s_waitcnt lgkmcnt(8)\n" \
"v_smfmac_f32_16x16x64_bf16 v[132:135], v[4:7], a[8:15], v182\n" \
"s_xor_b32 s29, s29, s37\n" \
"s_add_u32 s34, s21, 4096\n" \
"buffer_load_dwordx2 v[168:169], v170, s[8:11], s34 offen offset:3584 sc1\n" \
"v_cvt_pk_bf16_f32 v117, v86, v87\n" \
"v_smfmac_f32_16x16x64_bf16 v[136:139], v[4:7], a[88:95], v182\n" \
"ds_read_b128 v[0:3], v181 offset:0\n" \
"v_cvt_pk_bf16_f32 v120, v88, v89\n" \
"v_smfmac_f32_16x16x64_bf16 v[140:143], v[4:7], a[168:175], v182\n" \
"v_cvt_pk_bf16_f32 v121, v90, v91\n" \
"v_smfmac_f32_16x16x64_bf16 v[144:147], v[4:7], a[248:255], v182\n" \
"v_cvt_pk_bf16_f32 v124, v92, v93\n" \
"s_waitcnt lgkmcnt(8)\n" \
"v_smfmac_f32_16x16x64_bf16 v[132:135], v[8:11], a[16:23], v182\n" \
"v_cvt_pk_bf16_f32 v125, v94, v95\n" \
"v_smfmac_f32_16x16x64_bf16 v[136:139], v[8:11], a[96:103], v182\n" \
"ds_read_b128 v[4:7], v181 offset:1024\n" \
"v_smfmac_f32_16x16x64_bf16 v[140:143], v[8:11], a[176:183], v182\n" \
"v_smfmac_f32_16x16x64_bf16 v[144:147], v[8:11], v[192:199], v182\n" \
"s_waitcnt lgkmcnt(8)\n" \
"v_smfmac_f32_16x16x64_bf16 v[132:135], v[12:15], a[24:31], v182\n" \
"v_smfmac_f32_16x16x64_bf16 v[136:139], v[12:15], a[104:111], v182\n" \
"ds_read_b128 v[8:11], v181 offset:2048\n" \
"v_smfmac_f32_16x16x64_bf16 v[140:143], v[12:15], a[184:191], v182\n" \
"v_smfmac_f32_16x16x64_bf16 v[144:147], v[12:15], v[200:207], v182\n" \
"s_waitcnt lgkmcnt(8)\n" \
"v_smfmac_f32_16x16x64_bf16 v[132:135], v[16:19], a[32:39], v182\n" \
"v_smfmac_f32_16x16x64_bf16 v[136:139], v[16:19], a[112:119], v182\n" \
"ds_read_b128 v[12:15], v181 offset:3072\n" \
"v_smfmac_f32_16x16x64_bf16 v[140:143], v[16:19], a[192:199], v182\n" \
"v_smfmac_f32_16x16x64_bf16 v[144:147], v[16:19], v[208:215], v182\n" \
"s_waitcnt lgkmcnt(8)\n" \
"v_smfmac_f32_16x16x64_bf16 v[132:135], v[20:23], a[40:47], v182\n" \
"v_smfmac_f32_16x16x64_bf16 v[136:139], v[20:23], a[120:127], v182\n" \
"ds_read_b128 v[16:19], v181 offset:4096\n" \
"v_smfmac_f32_16x16x64_bf16 v[140:143], v[20:23], a[200:207], v182\n" \
"v_smfmac_f32_16x16x64_bf16 v[144:147], v[20:23], v[216:223], v182\n" \
"s_waitcnt lgkmcnt(8)\n" \
"v_smfmac_f32_16x16x64_bf16 v[132:135], v[24:27], a[48:55], v182\n" \
"v_smfmac_f32_16x16x64_bf16 v[136:139], v[24:27], a[128:135], v182\n" \
"ds_read_b128 v[20:23], v181 offset:5120\n" \
"v_smfmac_f32_16x16x64_bf16 v[140:143], v[24:27], a[208:215], v182\n" \
"v_smfmac_f32_16x16x64_bf16 v[144:147], v[24:27], v[224:231], v182\n" \
"s_waitcnt lgkmcnt(8)\n" \
"v_smfmac_f32_16x16x64_bf16 v[132:135], v[28:31], a[56:63], v182\n" \
"v_smfmac_f32_16x16x64_bf16 v[136:139], v[28:31], a[136:143], v182\n" \
"ds_read_b128 v[24:27], v181 offset:6144\n" \
"v_smfmac_f32_16x16x64_bf16 v[140:143], v[28:31], a[216:223], v182\n" \
"v_smfmac_f32_16x16x64_bf16 v[144:147], v[28:31], v[232:239], v182\n" \
"s_waitcnt lgkmcnt(8)\n" \
"v_smfmac_f32_16x16x64_bf16 v[132:135], v[32:35], a[64:71], v182\n" \
"v_smfmac_f32_16x16x64_bf16 v[136:139], v[32:35], a[144:151], v182\n" \
"ds_read_b128 v[28:31], v181 offset:7168\n" \
"v_smfmac_f32_16x16x64_bf16 v[140:143], v[32:35], a[224:231], v182\n" \
"v_smfmac_f32_16x16x64_bf16 v[144:147], v[32:35], v[240:247], v182\n" \
"s_waitcnt lgkmcnt(8)\n" \
"v_smfmac_f32_16x16x64_bf16 v[132:135], v[36:39], a[72:79], v182\n" \
"v_smfmac_f32_16x16x64_bf16 v[136:139], v[36:39], a[152:159], v182\n" \
"ds_read_b128 v[32:35], v181 offset:8192\n" \
"v_smfmac_f32_16x16x64_bf16 v[140:143], v[36:39], a[232:239], v182\n" \
"v_smfmac_f32_16x16x64_bf16 v[144:147], v[36:39], v[248:255], v182\n" \
"v_mfma_f32_16x16x32_bf16 v[96:99], v[68:71], a[68:71], v[96:99]\n" \
"v_mfma_f32_16x16x32_bf16 v[96:99], v[72:75], a[72:75], v[96:99]\n" \
"ds_read_b128 v[36:39], v181 offset:9216\n" \
"v_mfma_f32_16x16x32_bf16 v[96:99], v[76:79], a[76:79], v[96:99]\n" \
"v_permlane32_swap_b32 v132, v136\n" \
"v_permlane32_swap_b32 v133, v137\n" \
"v_mfma_f32_16x16x32_bf16 v[100:103], v[68:71], a[148:151], v[100:103]\n" \
"v_permlane32_swap_b32 v134, v138\n" \
"v_permlane32_swap_b32 v135, v139\n" \
"v_mfma_f32_16x16x32_bf16 v[100:103], v[72:75], a[152:155], v[100:103]\n" \
"v_permlane32_swap_b32 v140, v144\n" \
"v_permlane32_swap_b32 v141, v145\n" \
"v_mfma_f32_16x16x32_bf16 v[100:103], v[76:79], a[156:159], v[100:103]\n" \
"v_permlane32_swap_b32 v142, v146\n" \
"v_permlane32_swap_b32 v143, v147\n" \
"v_mfma_f32_16x16x32_bf16 v[104:107], v[68:71], a[228:231], v[104:107]\n" \
"v_add_f32 v132, v132, v136\n" \
"v_add_f32 v133, v133, v137\n" \
"v_mfma_f32_16x16x32_bf16 v[104:107], v[72:75], a[232:235], v[104:107]\n" \
"v_add_f32 v134, v134, v138\n" \
"v_add_f32 v135, v135, v139\n" \
"v_mfma_f32_16x16x32_bf16 v[104:107], v[76:79], a[236:239], v[104:107]\n" \
"v_cvt_pk_bf16_f32 v128, v132, v133\n" \
"v_cvt_pk_bf16_f32 v129, v134, v135\n" \
"v_mfma_f32_16x16x32_bf16 v[108:111], v[68:71], v[244:247], v[108:111]\n" \
"v_add_f32 v140, v140, v144\n" \
"v_add_f32 v141, v141, v145\n" \
"v_mfma_f32_16x16x32_bf16 v[108:111], v[72:75], v[248:251], v[108:111]\n" \
"v_add_f32 v142, v142, v146\n" \
"v_add_f32 v143, v143, v147\n" \
"v_mfma_f32_16x16x32_bf16 v[108:111], v[76:79], v[252:255], v[108:111]\n" \
"v_cvt_pk_bf16_f32 v130, v140, v141\n" \
"v_cvt_pk_bf16_f32 v131, v142, v143\n" \
"v_cvt_pk_bf16_f32 v114, v96, v97\n" \
"v_cvt_pk_bf16_f32 v115, v98, v99\n" \
"v_cvt_pk_bf16_f32 v118, v100, v101\n" \
"v_cvt_pk_bf16_f32 v119, v102, v103\n" \
"v_cvt_pk_bf16_f32 v122, v104, v105\n" \
"v_cvt_pk_bf16_f32 v123, v106, v107\n" \
"s_cmp_eq_u32 s18, 63\n" \
"s_cbranch_scc1 LR_last_soft\n" \
"v_cvt_pk_bf16_f32 v126, v108, v109\n" \
"v_cvt_pk_bf16_f32 v127, v110, v111\n" \
"s_mov_b32 s20, 262144\n" \
"LR_ts_soft:\n" \
"s_waitcnt vmcnt(0)\n" \
"v_min_i32 v166, v168, v169\n" \
"v_cmp_gt_i32 vcc, 0, v166\n" \
"s_and_b64 vcc, vcc, s[60:61]\n" \
"s_cbranch_vccnz LR_tsr\n" \
"LR_tsd_soft:\n" \
"s_cmp_eq_u32 s18, 3\n" \
"s_cbranch_scc1 LR_dec\n" \
"LR_dec_soft:\n" \
"v_max_i32 v168, v168, v169\n" \
"s_nop 1\n" \
"v_max_i32_dpp v168, v168, v168 row_shr:1 row_mask:0xf bank_mask:0xf\n" \
"s_nop 1\n" \
"v_max_i32_dpp v168, v168, v168 row_shr:2 row_mask:0xf bank_mask:0xf\n" \
"s_nop 1\n" \
"v_max_i32_dpp v168, v168, v168 row_shr:4 row_mask:0xf bank_mask:0xf\n" \
"s_nop 1\n" \
"v_max_i32_dpp v168, v168, v168 row_shr:8 row_mask:0xf bank_mask:0xf\n" \
"s_nop 0\n" \
"v_readlane_b32 s62, v168, 15\n" \
"v_readlane_b32 s63, v168, 31\n" \
"v_readlane_b32 s38, v168, 47\n" \
"v_readlane_b32 s39, v168, 63\n" \
"s_max_i32 s62, s62, s63\n" \
"s_max_i32 s38, s38, s39\n" \
"s_max_i32 s62, s62, s38\n" \
"s_add_u32 s62, s62, s31\n" \
"s_waitcnt lgkmcnt(0)\n" \
".p2align 5\n" \
"LR_tm:\n" \
"s_memrealtime s[38:39]\n" \
"s_waitcnt lgkmcnt(0)\n" \
"s_sub_u32 s38, s38, s62\n" \
"s_cmp_lt_i32 s38, 0\n" \
"s_cbranch_scc1 LR_tm\n" \
"buffer_load_dwordx4 v[132:135], v183, s[8:11], s21 offen offset:0 sc1\n" \
"buffer_load_dwordx4 v[136:139], v183, s[8:11], s21 offen offset:640 sc1\n" \
"buffer_load_dwordx4 v[140:143], v183, s[8:11], s21 offen offset:1280 sc1\n" \
"buffer_load_dwordx4 v[144:147], v183, s[8:11], s21 offen offset:1920 sc1\n" \
"buffer_load_dwordx2 v[148:149], v184, s[8:11], s21 offen offset:0 sc1\n" \
"buffer_load_dwordx2 v[150:151], v184, s[8:11], s21 offen offset:1280 sc1\n" \
"v_mfma_f32_16x16x32_bf16 v[80:83], v[0:3], a[0:3], 0\n" \
"ds_read_b128 v[40:43], v181 offset:10240\n" \
"v_mfma_f32_16x16x32_bf16 v[84:87], v[0:3], a[80:83], 0\n" \
"ds_read_b128 v[44:47], v181 offset:11264\n" \
"v_mfma_f32_16x16x32_bf16 v[88:91], v[0:3], a[160:163], 0\n" \
"ds_read_b128 v[48:51], v181 offset:12288\n" \
"v_mfma_f32_16x16x32_bf16 v[92:95], v[0:3], a[240:243], 0\n" \
"ds_read_b128 v[52:55], v181 offset:13312\n" \
"v_mfma_f32_16x16x32_bf16 v[80:83], v[4:7], a[4:7], v[80:83]\n" \
"ds_read_b128 v[56:59], v181 offset:14336\n" \
"v_mfma_f32_16x16x32_bf16 v[84:87], v[4:7], a[84:87], v[84:87]\n" \
"ds_read_b128 v[60:63], v181 offset:15360\n" \
"v_mfma_f32_16x16x32_bf16 v[88:91], v[4:7], a[164:167], v[88:91]\n" \
"ds_read_b128 v[64:67], v181 offset:16384\n" \
"v_mfma_f32_16x16x32_bf16 v[92:95], v[4:7], a[244:247], v[92:95]\n" \
"ds_read_b128 v[68:71], v181 offset:17408\n" \
"v_mfma_f32_16x16x32_bf16 v[80:83], v[8:11], a[8:11], v[80:83]\n" \
"ds_read_b128 v[72:75], v181 offset:18432\n" \
"v_mfma_f32_16x16x32_bf16 v[84:87], v[8:11], a[88:91], v[84:87]\n" \
"ds_read_b128 v[76:79], v181 offset:19456\n" \
"v_mfma_f32_16x16x32_bf16 v[88:91], v[8:11], a[168:171], v[88:91]\n" \
"v_mfma_f32_16x16x32_bf16 v[92:95], v[8:11], a[248:251], v[92:95]\n" \
"v_mfma_f32_16x16x32_bf16 v[80:83], v[12:15], a[12:15], v[80:83]\n" \
"v_mfma_f32_16x16x32_bf16 v[84:87], v[12:15], a[92:95], v[84:87]\n" \
"v_mfma_f32_16x16x32_bf16 v[88:91], v[12:15], a[172:175], v[88:91]\n" \
"v_mfma_f32_16x16x32_bf16 v[92:95], v[12:15], a[252:255], v[92:95]\n" \
"v_mfma_f32_16x16x32_bf16 v[80:83], v[16:19], a[16:19], v[80:83]\n" \
"v_mfma_f32_16x16x32_bf16 v[84:87], v[16:19], a[96:99], v[84:87]\n" \
"v_mfma_f32_16x16x32_bf16 v[88:91], v[16:19], a[176:179], v[88:91]\n" \
"v_mfma_f32_16x16x32_bf16 v[92:95], v[16:19], v[192:195], v[92:95]\n" \
"v_mfma_f32_16x16x32_bf16 v[80:83], v[20:23], a[20:23], v[80:83]\n" \
"v_mfma_f32_16x16x32_bf16 v[84:87], v[20:23], a[100:103], v[84:87]\n" \
"v_mfma_f32_16x16x32_bf16 v[88:91], v[20:23], a[180:183], v[88:91]\n" \
"v_mfma_f32_16x16x32_bf16 v[92:95], v[20:23], v[196:199], v[92:95]\n" \
"v_mfma_f32_16x16x32_bf16 v[80:83], v[24:27], a[24:27], v[80:83]\n" \
"v_mfma_f32_16x16x32_bf16 v[84:87], v[24:27], a[104:107], v[84:87]\n" \
"v_mfma_f32_16x16x32_bf16 v[88:91], v[24:27], a[184:187], v[88:91]\n" \
"v_mfma_f32_16x16x32_bf16 v[92:95], v[24:27], v[200:203], v[92:95]\n" \
"v_mfma_f32_16x16x32_bf16 v[80:83], v[28:31], a[28:31], v[80:83]\n" \
"v_mfma_f32_16x16x32_bf16 v[84:87], v[28:31], a[108:111], v[84:87]\n" \
"v_mfma_f32_16x16x32_bf16 v[88:91], v[28:31], a[188:191], v[88:91]\n" \
"v_mfma_f32_16x16x32_bf16 v[92:95], v[28:31], v[204:207], v[92:95]\n" \
"s_waitcnt lgkmcnt(0)\n" \
"s_mov_b32 s20, 262144\n" \
"LR_q:\n" \
"s_waitcnt vmcnt(5)\n" \
"v_mfma_f32_16x16x32_bf16 v[162:165], v[112:115], v[132:135], 0\n" \
"v_max3_u32 v166, v132, v133, v134\n" \
"v_max_u32 v166, v166, v135\n" \
"s_waitcnt vmcnt(4)\n" \
"v_mfma_f32_16x16x32_bf16 v[162:165], v[116:119], v[136:139], v[162:165]\n" \
"v_max3_u32 v166, v166, v136, v137\n" \
"v_max3_u32 v166, v166, v138, v139\n" \
"s_waitcnt vmcnt(3)\n" \
"v_mfma_f32_16x16x32_bf16 v[162:165], v[120:123], v[140:143], v[162:165]\n" \
"v_max3_u32 v166, v166, v140, v141\n" \
"v_max3_u32 v166, v166, v142, v143\n" \
"s_waitcnt vmcnt(2)\n" \
"v_mfma_f32_16x16x32_bf16 v[162:165], v[124:127], v[144:147], v[162:165]\n" \
"v_max3_u32 v166, v166, v144, v145\n" \
"v_max3_u32 v166, v166, v146, v147\n" \
"s_waitcnt vmcnt(0)\n" \
"v_mfma_f32_16x16x32_bf16 v[162:165], v[128:131], v[148:151], v[162:165]\n" \
"v_max3_u32 v166, v166, v148, v149\n" \
"v_max3_u32 v166, v166, v150, v151\n" \
"v_cmp_eq_u32 vcc, -1, v166\n" \
"s_cbranch_vccnz LR_qr\n" \
"LR_q_soft:\n" \
"s_mov_b64 exec, s[32:33]\n" \
"s_nop 2\n" \
"ds_write_b32 v187, v162 offset:0\n" \
"ds_write_b32 v187, v163 offset:16\n" \
"ds_write_b32 v187, v164 offset:32\n" \
"ds_write_b32 v187, v165 offset:48\n" \
"s_mov_b64 exec, -1\n" \
"s_waitcnt lgkmcnt(0)\n" \
"s_barrier\n" \
"s_cmp_lg_u32 s13, 0\n" \
"s_cbranch_scc1 LR_next\n" \
"ds_read_b128 v[168:171], v188 offset:10240\n" \
"ds_read_b128 v[172:175], v188 offset:11520\n" \
"s_waitcnt lgkmcnt(1)\n" \
"v_pk_add_f32 v[168:169], v[168:169], v[170:171]\n" \
"s_waitcnt lgkmcnt(0)\n" \
"v_pk_add_f32 v[172:173], v[172:173], v[174:175]\n" \
"v_pk_add_f32 v[168:169], v[168:169], v[172:173]\n" \
"v_add_f32 v168, v168, v169\n" \
"s_nop 1\n" \
"v_mov_b32_dpp v169, v168 quad_perm:[1,0,3,2] row_mask:0xf bank_mask:0xf\n" \
"s_mov_b64 exec, s[22:23]\n" \
"v_cvt_pk_bf16_f32 v168, v168, v169\n" \
"buffer_store_dword v168, v167, s[8:11], s30 offen sc1\n" \
"s_mov_b64 exec, -1\n" \
"LR_next:\n" \
"s_add_u32 s18, s18, 1\n" \
"s_mov_b32 s21, s30\n" \
"s_cmp_eq_u32 s18, 31\n" \
"s_cselect_b32 s35, s14, 0\n" \
"s_cmp_eq_u32 s18, 4\n" \
"s_cselect_b32 s35, s15, s35\n" \
"s_add_u32 s35, s35, 65536\n" \
"s_add_u32 s30, s30, s35\n" \
"s_branch LR_loop\n" \
"LR_last_soft:\n" \
"s_nop 7\n" \
"v_cvt_pk_bf16_f32 v126, v108, v109\n" \
"v_cvt_pk_bf16_f32 v127, v110, v111\n" \
"s_nop 1\n" \
"buffer_store_dwordx4 v[112:115], v185, s[8:11], s25 offen offset:0 sc1\n" \
"buffer_store_dwordx4 v[116:119], v185, s[8:11], s25 offen offset:1024 sc1\n" \
"buffer_store_dwordx4 v[120:123], v185, s[8:11], s25 offen offset:2048 sc1\n" \
"buffer_store_dwordx4 v[124:127], v185, s[8:11], s25 offen offset:3072 sc1\n" \
"buffer_store_dwordx4 v[128:131], v185, s[8:11], s26 offen sc1\n" \
"s_waitcnt vmcnt(0) lgkmcnt(0)\n" \
"s_barrier\n" \
"s_cmp_eq_u32 s13, 0\n" \
"s_cselect_b64 exec, 1, 0\n" \
"buffer_store_dword v189, off, s[8:11], s27 sc1\n" \
"s_endpgm\n" \
"LR_tsr:\n" \
"s_cmp_lg_u32 s19, 0\n" \
"s_cbranch_scc1 LR_tsd_soft\n" \
"s_sleep 2\n" \
"buffer_load_dwordx2 v[168:169], v170, s[8:11], s34 offen offset:3584 sc1\n" \
"s_sub_u32 s20, s20, 1\n" \
"s_cselect_b32 s19, 1, 0\n" \
"s_branch LR_ts_soft\n" \
"LR_dec:\n" \
"s_mov_b32 s20, 262144\n" \
"LR_dl:\n" \
"s_nop 1\n" \
"v_readlane_b32 s35, v168, 8\n" \
"s_cmp_lg_u32 s35, -1\n" \
"s_cbranch_scc1 LR_dok\n" \
"s_cmp_lg_u32 s19, 0\n" \
"s_cbranch_scc1 LR_dok\n" \
"s_sleep 4\n" \
"buffer_load_dwordx2 v[168:169], v170, s[8:11], s34 offen offset:3584 sc1\n" \
"s_sub_u32 s20, s20, 1\n" \
"s_cselect_b32 s19, 1, 0\n" \
"s_waitcnt vmcnt(0)\n" \
"s_branch LR_dl\n" \
"LR_dok:\n" \
"s_and_b32 s15, s35, 1\n" \
"s_lshl_b32 s15, s15, 13\n" \
"s_and_b32 s14, s35, 2\n" \
"s_lshl_b32 s14, s14, 12\n" \
"s_sub_u32 s14, s14, s15\n" \
"s_branch LR_dec_soft\n" \
"LR_qr:\n" \
"s_nop 7\n" \
"s_cmp_lg_u32 s19, 0\n" \
"s_cbranch_scc1 LR_q_soft\n" \
"s_mov_b64 exec, vcc\n" \
"buffer_load_dwordx4 v[132:135], v183, s[8:11], s21 offen offset:0 sc1\n" \
"buffer_load_dwordx4 v[136:139], v183, s[8:11], s21 offen offset:640 sc1\n" \
"buffer_load_dwordx4 v[140:143], v183, s[8:11], s21 offen offset:1280 sc1\n" \
"buffer_load_dwordx4 v[144:147], v183, s[8:11], s21 offen offset:1920 sc1\n" \
"buffer_load_dwordx2 v[148:149], v184, s[8:11], s21 offen offset:0 sc1\n" \
"buffer_load_dwordx2 v[150:151], v184, s[8:11], s21 offen offset:1280 sc1\n" \
"s_mov_b64 exec, -1\n" \
"s_sub_u32 s20, s20, 1\n" \
"s_cselect_b32 s19, 1, 0\n" \
"s_branch LR_q\n" \
"LR_mslow:\n" \
"s_cmp_lg_u32 s19, 0\n" \
"s_cbranch_scc1 LR_m_to\n" \
"s_mov_b32 s20, 262144\n" \
"LR_m_012:\n" \
"s_waitcnt vmcnt(2)\n" \
"v_max3_u32 v166, v152, v153, v154\n" \
"v_max_u32 v166, v166, v155\n" \
"v_cmp_eq_u32 vcc, -1, v166\n" \
"s_cbranch_vccnz LR_m_012_r\n" \
"ds_write_b128 v190, v[152:155] offset:0\n" \
"LR_m_12:\n" \
"s_waitcnt vmcnt(1)\n" \
"v_max3_u32 v166, v156, v157, v158\n" \
"v_max_u32 v166, v166, v159\n" \
"v_cmp_eq_u32 vcc, -1, v166\n" \
"s_cbranch_vccnz LR_m_12_r\n" \
"ds_write_b128 v190, v[156:159] offset:1024\n" \
"LR_m_2:\n" \
"s_waitcnt vmcnt(0)\n" \
"v_max_u32 v166, v160, v161\n" \
"v_cmp_eq_u32 vcc, -1, v166\n" \
"s_cbranch_vccnz LR_m_2_r\n" \
"ds_write_b64 v191, v[160:161] offset:0\n" \
"s_branch LR_m_done\n" \
"LR_m_120:\n" \
"s_waitcnt vmcnt(2)\n" \
"v_max3_u32 v166, v156, v157, v158\n" \
"v_max_u32 v166, v166, v159\n" \
"v_cmp_eq_u32 vcc, -1, v166\n" \
"s_cbranch_vccnz LR_m_120_r\n" \
"ds_write_b128 v190, v[156:159] offset:1024\n" \
"s_branch LR_m_20\n" \
"LR_m_201:\n" \
"s_waitcnt vmcnt(2)\n" \
"v_max_u32 v166, v160, v161\n" \
"v_cmp_eq_u32 vcc, -1, v166\n" \
"s_cbranch_vccnz LR_m_201_r\n" \
"ds_write_b64 v191, v[160:161] offset:0\n" \
"s_branch LR_m_01\n" \
"LR_m_01:\n" \
"s_waitcnt vmcnt(1)\n" \
"v_max3_u32 v166, v152, v153, v154\n" \
"v_max_u32 v166, v166, v155\n" \
"v_cmp_eq_u32 vcc, -1, v166\n" \
"s_cbranch_vccnz LR_m_01_r\n" \
"ds_write_b128 v190, v[152:155] offset:0\n" \
"s_branch LR_m_1\n" \
"LR_m_10:\n" \
"s_waitcnt vmcnt(1)\n" \
"v_max3_u32 v166, v156, v157, v158\n" \
"v_max_u32 v166, v166, v159\n" \
"v_cmp_eq_u32 vcc, -1, v166\n" \
"s_cbranch_vccnz LR_m_10_r\n" \
"ds_write_b128 v190, v[156:159] offset:1024\n" \
"s_branch LR_m_0\n" \
"LR_m_02:\n" \
"s_waitcnt vmcnt(1)\n" \
"v_max3_u32 v166, v152, v153, v154\n" \
"v_max_u32 v166, v166, v155\n" \
"v_cmp_eq_u32 vcc, -1, v166\n" \
"s_cbranch_vccnz LR_m_02_r\n" \
"ds_write_b128 v190, v[152:155] offset:0\n" \
"s_branch LR_m_2\n" \
"LR_m_20:\n" \
"s_waitcnt vmcnt(1)\n" \
"v_max_u32 v166, v160, v161\n" \
"v_cmp_eq_u32 vcc, -1, v166\n" \
"s_cbranch_vccnz LR_m_20_r\n" \
"ds_write_b64 v191, v[160:161] offset:0\n" \
"s_branch LR_m_0\n" \
"LR_m_21:\n" \
"s_waitcnt vmcnt(1)\n" \
"v_max_u32 v166, v160, v161\n" \
"v_cmp_eq_u32 vcc, -1, v166\n" \
"s_cbranch_vccnz LR_m_21_r\n" \
"ds_write_b64 v191, v[160:161] offset:0\n" \
"s_branch LR_m_1\n" \
"LR_m_0:\n" \
"s_waitcnt vmcnt(0)\n" \
"v_max3_u32 v166, v152, v153, v154\n" \
"v_max_u32 v166, v166, v155\n" \
"v_cmp_eq_u32 vcc, -1, v166\n" \
"s_cbranch_vccnz LR_m_0_r\n" \
"ds_write_b128 v190, v[152:155] offset:0\n" \
"s_branch LR_m_done\n" \
"LR_m_1:\n" \
"s_waitcnt vmcnt(0)\n" \
"v_max3_u32 v166, v156, v157, v158\n" \
"v_max_u32 v166, v166, v159\n" \
"v_cmp_eq_u32 vcc, -1, v166\n" \
"s_cbranch_vccnz LR_m_1_r\n" \
"ds_write_b128 v190, v[156:159] offset:1024\n" \
"s_branch LR_m_done\n" \
"LR_m_012_r:\n" \
"s_mov_b64 exec, vcc\n" \
"buffer_load_dwordx4 v[152:155], v176, s[8:11], s21 offen sc1\n" \
"s_mov_b64 exec, -1\n" \
"s_sub_u32 s20, s20, 1\n" \
"s_cbranch_scc0 LR_m_120\n" \
"s_branch LR_m_to\n" \
"LR_m_12_r:\n" \
"s_mov_b64 exec, vcc\n" \
"buffer_load_dwordx4 v[156:159], v176, s[8:11], s21 offen offset:1024 sc1\n" \
"s_mov_b64 exec, -1\n" \
"s_sub_u32 s20, s20, 1\n" \
"s_cbranch_scc0 LR_m_21\n" \
"s_branch LR_m_to\n" \
"LR_m_2_r:\n" \
"s_mov_b64 exec, vcc\n" \
"buffer_load_dwordx2 v[160:161], v177, s[8:11], s21 offen sc1\n" \
"s_mov_b64 exec, -1\n" \
"s_sub_u32 s20, s20, 1\n" \
"s_cbranch_scc0 LR_m_2\n" \
"s_branch LR_m_to\n" \
"LR_m_120_r:\n" \
"s_mov_b64 exec, vcc\n" \
"buffer_load_dwordx4 v[156:159], v176, s[8:11], s21 offen offset:1024 sc1\n" \
"s_mov_b64 exec, -1\n" \
"s_sub_u32 s20, s20, 1\n" \
"s_cbranch_scc0 LR_m_201\n" \
"s_branch LR_m_to\n" \
"LR_m_201_r:\n" \
"s_mov_b64 exec, vcc\n" \
"buffer_load_dwordx2 v[160:161], v177, s[8:11], s21 offen sc1\n" \
"s_mov_b64 exec, -1\n" \
"s_sub_u32 s20, s20, 1\n" \
"s_cbranch_scc0 LR_m_012\n" \
"s_branch LR_m_to\n" \
"LR_m_01_r:\n" \
"s_mov_b64 exec, vcc\n" \
"buffer_load_dwordx4 v[152:155], v176, s[8:11], s21 offen sc1\n" \
"s_mov_b64 exec, -1\n" \
"s_sub_u32 s20, s20, 1\n" \
"s_cbranch_scc0 LR_m_10\n" \
"s_branch LR_m_to\n" \
"LR_m_10_r:\n" \
"s_mov_b64 exec, vcc\n" \
"buffer_load_dwordx4 v[156:159], v176, s[8:11], s21 offen offset:1024 sc1\n" \
"s_mov_b64 exec, -1\n" \
"s_sub_u32 s20, s20, 1\n" \
"s_cbranch_scc0 LR_m_01\n" \
"s_branch LR_m_to\n" \
"LR_m_02_r:\n" \
"s_mov_b64 exec, vcc\n" \
"buffer_load_dwordx4 v[152:155], v176, s[8:11], s21 offen sc1\n" \
"s_mov_b64 exec, -1\n" \
"s_sub_u32 s20, s20, 1\n" \
"s_cbranch_scc0 LR_m_20\n" \
"s_branch LR_m_to\n" \
"LR_m_20_r:\n" \
"s_mov_b64 exec, vcc\n" \
"buffer_load_dwordx2 v[160:161], v177, s[8:11], s21 offen sc1\n" \
"s_mov_b64 exec, -1\n" \
"s_sub_u32 s20, s20, 1\n" \
"s_cbranch_scc0 LR_m_02\n" \
"s_branch LR_m_to\n" \
"LR_m_21_r:\n" \
"s_mov_b64 exec, vcc\n" \
"buffer_load_dwordx2 v[160:161], v177, s[8:11], s21 offen sc1\n" \
"s_mov_b64 exec, -1\n" \
"s_sub_u32 s20, s20, 1\n" \
"s_cbranch_scc0 LR_m_12\n" \
"s_branch LR_m_to\n" \
"LR_m_0_r:\n" \
"s_mov_b64 exec, vcc\n" \
"buffer_load_dwordx4 v[152:155], v176, s[8:11], s21 offen sc1\n" \
"s_mov_b64 exec, -1\n" \
"s_sub_u32 s20, s20, 1\n" \
"s_cbranch_scc0 LR_m_0\n" \
"s_branch LR_m_to\n" \
"LR_m_1_r:\n" \
"s_mov_b64 exec, vcc\n" \
"buffer_load_dwordx4 v[156:159], v176, s[8:11], s21 offen offset:1024 sc1\n" \
"s_mov_b64 exec, -1\n" \
"s_sub_u32 s20, s20, 1\n" \
"s_cbranch_scc0 LR_m_1\n" \
"s_branch LR_m_to\n" \
"LR_m_to:\n" \
"s_mov_b32 s19, 1\n" \
"s_waitcnt vmcnt(0)\n" \
"LR_m_done:\n" \
"s_waitcnt lgkmcnt(0)\n" \
"s_branch LR_m_soft\n"
#define S_WOPS "{a[0:7]}"(W[0][0]), "{a[8:15]}"(W[1][0]), "{a[16:23]}"(W[2][0]), "{a[24:31]}"(W[3][0]), "{a[32:39]}"(W[4][0]), "{a[40:47]}"(W[5][0]), "{a[48:55]}"(W[0][1]), "{a[56:63]}"(W[1][1]), "{a[64:71]}"(W[2][1]), "{a[72:79]}"(W[3][1]), "{a[80:87]}"(W[4][1]), "{a[88:95]}"(W[5][1]), "{a[96:103]}"(W[0][2]), "{a[104:111]}"(W[1][2]), "{a[112:119]}"(W[2][2]), "{a[120:127]}"(W[3][2]), "{a[128:135]}"(W[4][2]), "{a[136:143]}"(W[5][2]), "{a[144:151]}"(W[0][3]), "{a[152:159]}"(W[1][3]), "{a[160:167]}"(W[2][3]), "{a[168:175]}"(W[3][3]), "{a[176:183]}"(W[4][3]), "{a[184:191]}"(W[5][3]), "{a[192:199]}"(W[0][4]), "{a[200:207]}"(W[1][4]), "{a[208:215]}"(W[2][4]), "{a[216:223]}"(W[3][4]), "{a[224:231]}"(W[4][4]), "{a[232:239]}"(W[5][4]), "{a[240:247]}"(W[0][5]), "{a[248:255]}"(W[1][5]), "{v[128:135]}"(W[2][5]), "{v[136:143]}"(W[3][5]), "{v[144:151]}"(W[4][5]), "{v[152:159]}"(W[5][5]), "{v[160:167]}"(W[0][6]), "{v[168:175]}"(W[1][6]), "{v[176:183]}"(W[2][6]), "{v[184:191]}"(W[3][6]), "{v[192:199]}"(W[4][6]), "{v[200:207]}"(W[5][6]), "{v[208:215]}"(W[0][7]), "{v[216:223]}"(W[1][7]), "{v[224:231]}"(W[2][7]), "{v[232:239]}"(W[3][7]), "{v[240:247]}"(W[4][7]), "{v[248:255]}"(W[5][7])
#define R_WOPS "{a[0:7]}"(D[0][0]), "{a[8:15]}"(D[0][1]), "{a[16:23]}"(D[0][2]), "{a[24:31]}"(D[0][3]), "{a[32:39]}"(D[0][4]), "{a[40:47]}"(D[0][5]), "{a[48:55]}"(D[0][6]), "{a[56:63]}"(D[0][7]), "{a[64:71]}"(D[0][8]), "{a[72:79]}"(D[0][9]), "{a[80:87]}"(D[1][0]), "{a[88:95]}"(D[1][1]), "{a[96:103]}"(D[1][2]), "{a[104:111]}"(D[1][3]), "{a[112:119]}"(D[1][4]), "{a[120:127]}"(D[1][5]), "{a[128:135]}"(D[1][6]), "{a[136:143]}"(D[1][7]), "{a[144:151]}"(D[1][8]), "{a[152:159]}"(D[1][9]), "{a[160:167]}"(D[2][0]), "{a[168:175]}"(D[2][1]), "{a[176:183]}"(D[2][2]), "{a[184:191]}"(D[2][3]), "{a[192:199]}"(D[2][4]), "{a[200:207]}"(D[2][5]), "{a[208:215]}"(D[2][6]), "{a[216:223]}"(D[2][7]), "{a[224:231]}"(D[2][8]), "{a[232:239]}"(D[2][9]), "{a[240:247]}"(D[3][0]), "{a[248:255]}"(D[3][1]), "{v[192:199]}"(D[3][2]), "{v[200:207]}"(D[3][3]), "{v[208:215]}"(D[3][4]), "{v[216:223]}"(D[3][5]), "{v[224:231]}"(D[3][6]), "{v[232:239]}"(D[3][7]), "{v[240:247]}"(D[3][8]), "{v[248:255]}"(D[3][9])
#define SWSTEP 5
#define SCLOB "s14", "s16", "s17", "s18", "s19", "s20", "s21", "s22", "s23", "s24", "s25", "s26", "s27", "s28", "s29", "s30", "s31", "s32", "s33", "s34", "s35", "s37", "s38", "s39", "s40", "s41", "s42", "s43", "s44", "s45", "s46", "s47", "s48", "s49", "s50", "s51", "s52", "s53", "s54", "s55", "s56", "s57", "s58", "s59", "s60", "s61", "s62", "s63", "s64", "s65", "s66", "s67", "s68", "s69", "s70", "s71", "s72", "s73", "s74", "s75", "s76", "s77", "s78", "s79", "s80", "s81", "s82", "s83", "s84", "s85", "s86", "s87", "s88", "s89", "s90", "s91", "vcc", "memory"
#define TS(step, slot)
#define TC(step, slot)
#define PFD 6
#define OSL 4
#define OEARLY 60
#define PFS 16
#ifndef NVOTE
#define NVOTE 4
#endif
#define TDEL 250
#define VDEL 1100
#define DSL 6
#define XP 0
#define XL 4
#define GP 0
#define GL 4
#define GSW 0
#define XROLL 1
#define PFK 0
#define CPF 0
#define HS 0
#define HX 0
#define G2DEL GDEL
#define DDEP 0
#define RDEL 0
#define GDEL 600
#define QDEL 0
DEV void pace(unsigned t0, int d) {
  for (int i = 0; i < 512 && (int)((unsigned)__builtin_amdgcn_s_memrealtime() - t0) < d; i++) __builtin_amdgcn_s_sleep(4);
}
struct Ctx { lchar* L; char* ws; rsrc_t rs; u4 srd, srdp; unsigned lb, pofs, cb, pd, tb; int tid, l, w, id; };

template <int C> DEV float dadd(float v) { return v + u2f(__builtin_amdgcn_update_dpp(0, f2u(v), C, 0xf, 0xf, true)); }
DEV float wsum(float v) {
  v = dadd<0xB1>(v);
  v = dadd<0x4E>(v);
  v = dadd<0x141>(v);
  v = dadd<0x140>(v);
  return xsum32(xsum16(v));
}
DEV int swap13(int r) { return (r & ~10) | ((r >> 2) & 2) | ((r << 2) & 8); }

template <int NK, int SC1> DEV void lin_dma(const Ctx& c, unsigned soff, unsigned ldsoff) {
  const unsigned v = c.w * (NK * 1024) + c.l * 16;
  const unsigned m = c.lb + ldsoff + c.w * (NK * 1024);
#pragma unroll
  for (int i = 0; i < NK; i += 4) {
    bdma<0, SC1>(c.srd, v, soff + i * 1024, m + i * 1024);
    if (i + 1 < NK) bdma<1024, SC1>(c.srd, v, soff + i * 1024, m + i * 1024);
    if (i + 2 < NK) bdma<2048, SC1>(c.srd, v, soff + i * 1024, m + i * 1024);
    if (i + 3 < NK) bdma<3072, SC1>(c.srd, v, soff + i * 1024, m + i * 1024);
  }
}

#define CODE_PF(TAG, part, nparts, ldsoff) do { \
  unsigned long long pc_; unsigned sz_; \
  asm volatile("s_getpc_b64 %0\n.Lpf_beg" TAG ":\n\ts_mov_b32 %1, .Lpf_end" TAG "-.Lpf_beg" TAG : "=&s"(pc_), "=s"(sz_)); \
  if (sz_ > 262144) sz_ = 0; \
  for (unsigned o_ = (part) * 1024; o_ + 1024 <= sz_; o_ += (nparts) * 1024) gdma((const char*)(pc_ & ~15ull) + o_ + c.l * 16, c.lb + (ldsoff)); \
} while (0)
#define CODE_END(TAG) asm volatile(".Lpf_end" TAG ":")

constexpr int IMG_QK = 53248;
DEV void qk_dma(const Ctx& c, const u16* Wq, const u16* Wk, int h, int ks, unsigned buf) {
  const int kb = ks < 8 ? ks + (ks + 2) / 4 : 5 * ks - 38;
#pragma unroll
  for (int i = 0; i < 13; i++) {
    const int slot = i * 256 + c.tid;
    const int row = slot / 26, cc = slot - row * 26;
    const int k = 64 * kb + swap13(row & 63);
    const u16* src = ((row >> 6) ? Wk : Wq) + (size_t)k * 6144 + h * 192 + (cc < 24 ? cc : 23) * 8;
    gdma(src, c.lb + buf + (i * 256 + c.w * 64) * 16);
  }
}
DEV u8v qk_frag(lchar* lb, int tt) {
  const int o = (tt / 3) * 64 * 416 + (tt % 3) * 32;
  u2 a = trread(lb + o), b = trread(lb + o + 8 * 416), cc = trread(lb + o + 32 * 416), d = trread(lb + o + 40 * 416);
  u8v r = {a[0], a[1], b[0], b[1], cc[0], cc[1], d[0], d[1]};
  return r;
}
DEV int qk_lanebase(int l, int w) {
  const int gb = l >> 4, q = (l >> 2) & 3, p = l & 3;
  return (16 * (gb >> 1) + 4 * (gb & 1) + q) * 416 + (48 * w + 4 * p) * 2;
}
template <int TOA> DEV void qk_load(const Ctx& c, const u16* Wq, const u16* Wk, int h, u8v (&W)[6][8]) {
  const int lb = qk_lanebase(c.l, c.w);
  qk_dma(c, Wq, Wk, h, 0, 0);
  qk_dma(c, Wq, Wk, h, 1, IMG_QK);
  qk_dma(c, Wq, Wk, h, 2, 2 * IMG_QK);
#pragma unroll
  for (int kb = 0; kb < 8; kb++) {
    wait_vm<26>();
    bar();
#pragma unroll
    for (int tt = 0; tt < 6; tt++) {
      u8v f = qk_frag(c.L + (kb % 3) * IMG_QK + lb, tt);
      W[tt][kb] = (TOA && kb * 6 + tt < 28) ? toA(f) : f;
    }
    bar();
    if (kb + 3 < 10) qk_dma(c, Wq, Wk, h, kb + 3, (kb % 3) * IMG_QK);
  }
  wait_vm0();
  bar();
}

constexpr float SCALE = 0.07216878364870322f;

DEV void role_S(const Ctx& c, int h, const u16* Wq, const u16* Wk) {
  u8v W[6][8];
  qk_load<0>(c, Wq, Wk, h, W);
  const int l = c.l, g = l >> 4, n = l & 15, w = c.w;
  {
    lchar* w8 = c.L + 2 * IMG_QK + qk_lanebase(c.l, c.w);
    lchar* w9 = c.L + qk_lanebase(c.l, c.w);
    lchar* wf = c.L + 61440 + w * 24576 + l * 16;
#pragma unroll
    for (int kbi = 0; kbi < 2; kbi++) {
      u8v f[6];
#pragma unroll
      for (int i = 0; i < 6; i++) f[i] = qk_frag(kbi ? w9 : w8, i);
      bar();
#pragma unroll
      for (int i = 0; i < 6; i++) {
        lds_w128(wf + (kbi * 6 + i) * 2048, (u4){f[i][0], f[i][1], f[i][2], f[i][3]});
        lds_w128(wf + (kbi * 6 + i) * 2048 + 1024, (u4){f[i][4], f[i][5], f[i][6], f[i][7]});
      }
    }
    wait_lgkm0();
  }
  const unsigned trb = 10240 + (4 * g + ((l >> 2) & 3)) * 32;
  const int qi = 16 * (n >> 3) + 8 * (w - 1) + (n & 7) - 16 * (g >> 1) - 4 * (g & 1);
  asm volatile(S_ASM :: S_WOPS, "{v112}"(w * 16384 + 5120 + (w & 1) * 512 + l * 16), "{v113}"(w * 16384 + 7168 - (w & 1) * 2048 + l * 8), "{v114}"(l * 16), "{v115}"(61440 + w * 24576 + l * 16),
               "{v116}"((l & 8) ? 0xEEEE : 0x4444), "{v117}"(10240 + (48 * w + n) * 32 + 8 * (g & 1) + 4 * (g >> 1)),
               "{v118}"(trb + (l & 3) * 8), "{v119}"(trb + 16 + (l & 1) * 8), "{v120}"(g * 128 + n * 16), "{v121}"(512 + (g & 1) * 64 + n * 8),
               "{v124}"(w * 2560 + (w & 1) * 512 + l * 16), "{v125}"(w * 2560 + 2048 - (w & 1) * 2048 + l * 8), "{v126}"(l * 8), "{v127}"(qi),
               "{s[4:7]}"(c.srdp), "{s[8:11]}"(c.srd), "{s2}"((h >> 3) * 16384 + 7680 + (h & 7) * 4), "{s3}"(c.tb), "{s12}"(h), "{s13}"(w), "{s15}"(c.cb), "{s36}"(c.pofs)
               : SCLOB);
}

template <int U0, int U1> DEV void a_dma(const Ctx& c, const u16* pv) {
  if (c.w == 3) return;
#pragma unroll
  for (int u = U0; u < U1; u++)
#pragma unroll
    for (int i = 0; i < 4; i++) {
      const int slot = i * 192 + c.tid, r = slot / 24, p = slot - r * 24;
      gdma(pv + (size_t)((u & 1) * 160 + (u >> 1) * 32 + r) * 6144 + (p ^ ((r >> 2) & 7)) * 8, c.lb + u * 12288 + (i * 192 + c.w * 64) * 16);
    }
}
#define NU0 7
#define NU1 10
__global__ void __launch_bounds__(256, 1) k_prep(const u16* __restrict__ x, const u16* __restrict__ mem0, const u16* __restrict__ Wv,
                                                 const u16* __restrict__ Wo, const u16* __restrict__ Wq, const u16* __restrict__ Wk,
                                                 char* __restrict__ ws) {
  __shared__ __attribute__((aligned(16))) char smem[159744 + 1024];
  const int tid = threadIdx.x, l = tid & 63, w = __builtin_amdgcn_readfirstlane(tid >> 6);
  const int pb = blockIdx.x, bi = pb >> 3, h = (pb & 7) * 4 + (bi >> 3), rb = (bi >> 2) & 1, cq = bi & 3;
  Ctx c;
  c.L = (lchar*)smem;
  c.lb = (unsigned)(uintptr_t)c.L;
  c.ws = ws;
  c.rs = __builtin_amdgcn_make_buffer_rsrc(ws, 0, WS_USED, 0x00020000);
  c.w = w; c.l = l; c.tid = tid; c.id = pb;
  TS(66, 0);
  if ((CPF & 1) && w == 3) CODE_PF("0", (pb >> 3) & 3, 4, 159744);
  constexpr unsigned LO = 98304, LX = 86016;
  const u16* pv = Wv + h * 192 + (size_t)rb * (320 * 6144);
  if (w < 3) {
#pragma unroll
    for (int i = 0; i < 20; i++) {
      const int slot = i * 192 + tid, r = slot / 20, p = slot - r * 20;
      gdma(Wo + ((size_t)h * 192 + r) * 640 + cq * 160 + (p ^ ((r >> 2) & 2)) * 8, c.lb + LO + (i * 192 + w * 64) * 16);
    }
    TS(66, 2);
    a_dma<0, NU0>(c, pv);
    TS(66, 4);
  } else {
    const u16* px = x + (size_t)(pb * 8 + (l & 7)) * 640 + (l >> 3) * 16;
#pragma unroll
    for (int i = 0; i < 10; i++) gdma(px + (i >> 1) * 128 + (i & 1) * 8, c.lb + LX + i * 1024);
    {
      const unsigned cb = comm_base(ws) + pb * 16384;
      const u4 sv = {SENT, SENT, SENT, SENT}, zv = {0, 0, 0, 0};
#pragma unroll
      for (int k = 0; k < 16; k++) {
        const unsigned in8 = ((k & 7) * 64 + l) * 16;
        if (in8 < 7808 && !(pb < 4 && in8 >= 5120 && in8 < 7680)) st128(sv, c.rs, in8, cb + (k >> 3) * 8192);
      }
      if (l < 32) *(u4*)(ws + OFF_FLAG + (size_t)(pb * 32 + l) * 16) = zv;
      else if (l < 41) { if (pb * 9 + l - 32 < 2048 + 80) *(unsigned*)(ws + OFF_PXF + (size_t)(pb * 9 + l - 32) * 128) = 0; }
      else if (l == 41) { if (pb < 128) *(u4*)(ws + OFF_TICK + (size_t)pb * 16) = zv; }
      else if (l == 42) { if (pb == 255) *(unsigned*)(ws + OFF_VOTE + 1024) = (unsigned)__builtin_amdgcn_s_memrealtime(); }
      CB();
    }
    TS(66, 3);
    wait_vm<13>();
    TS(66, 5);
    float v[80];
#pragma unroll
    for (int i = 0; i < 10; i++) {
      const u4 d = lds_r128(c.L + LX + i * 1024 + l * 16);
#pragma unroll
      for (int k = 0; k < 4; k++) { v[8 * i + 2 * k] = bflo(d[k]); v[8 * i + 2 * k + 1] = bfhi(d[k]); }
    }
    float s = 0;
#pragma unroll
    for (int i = 0; i < 80; i++) s += v[i];
    const float mean = xsum32(xsum16(dadd<0x128>(s))) * (1.0f / 640.0f);
    float q = 0;
#pragma unroll
    for (int i = 0; i < 80; i++) { float d = v[i] - mean; q += d * d; }
    q = xsum32(xsum16(dadd<0x128>(q)));
    const float mb = bfr(mean);
    const float vb = bfr(q * (1.0f / 640.0f));
    const float ve = bfr(vb + 1e-5f);
    const float rs = 1.0f / bfr(sqrtf(ve));
    const unsigned vo = (l >> 4) * 1024 + (((l >> 3) & 1) * 32 + ((pb >> 1) & 1) * 8 + (l & 7)) * 16;
    const unsigned so = OFF_XNP + ((pb >> 2) * 2 + (pb & 1)) * 20480;
#pragma unroll
    for (int i = 0; i < 5; i++) {
      unsigned o[8];
#pragma unroll
      for (int k = 0; k < 8; k++) o[k] = pack2(bfr(v[16 * i + 2 * k] - mb) * rs, bfr(v[16 * i + 2 * k + 1] - mb) * rs);
      st128((u4){o[0], o[4], o[1], o[5]}, c.rs, vo, so + i * 4096);
      st128((u4){o[2], o[6], o[3], o[7]}, c.rs, vo + 256, so + i * 4096);
    }
    CB();
    TS(66, 1);
  }
  const int m = l & 15, g = l >> 4, wr = w >> 1, wc = w & 1;
  const int mg = m >> 2, mi = m & 3;
  const int din = 16 * (mg >> 1) + 4 * (mg & 1) + 8 * (mi >> 1) + (mi & 1);
  const int sw = (din >> 2) & 7;
  lchar* pa0 = c.L + wr * 12288 + din * 384 + ((g ^ sw) * 16), *pa1 = c.L + wr * 12288 + din * 384 + (((g + 4) ^ sw) * 16);
  if (w < 3) wait_vm<4 * NU0>();
  TS(66, 6);
  bar();
  TS(65, 0);
  u4 B[6][5];
  {
    const int qq = (l >> 2) & 3, p = l & 3;
    lchar* L = c.L + LO + (8 * g + qq) * 320 + p * 8;
#pragma unroll
    for (int u = 0; u < 6; u++)
#pragma unroll
      for (int nt = 0; nt < 5; nt++) {
        lchar* q = L + u * (32 * 320) + ((5 * wc + nt) ^ (g & 1)) * 32;
        u2 b0 = trread(q), b1 = trread(q + 4 * 320);
        B[u][nt] = toA4((u4){b0[0], b0[1], b1[0], b1[1]});
      }
  }
  bar();
  a_dma<NU0, NU1>(c, pv);
  if (pb < 4 && tid < 160) {
    const unsigned L = pb * 2560 + tid * 16;
    u4 d = *(const u4*)(mem0 + ((L >> 4) & 7) * 640 + (L >> 7) * 8);
    d[0] = nosent(d[0]); d[1] = nosent(d[1]); d[2] = nosent(d[2]); d[3] = nosent(d[3]);
    const unsigned cb = comm_base(ws) + pb * 16384 + 5120;
    st128(d, c.rs, tid * 16, cb);
    st128(d, c.rs, tid * 16, cb + 8192);
    CB();
  }
  const unsigned vo = (m >> 3) * DJS + ((h >> 4) * 8 + (m & 7) + 16 * g) * 16;
  const unsigned so = OFF_D + (cq * 10 + wc * 5) * (2 * DJS) + ((h >> 2) & 3) * DWVS + ((h & 3) * 20 + rb * 10 + wr * 5) * 1024;
  u4 o[5];
#pragma unroll
  for (int k = 0; k < 5; k++) {
    if (k == 0) wait_vm<4 * (NU1 - 2)>(); else if (k == 1) wait_vm<24>(); else if (k == 2) wait_vm<16 + 5>(); else if (k == 3) wait_vm<8 + 10>(); else wait_vm<15>();
    bar();
    u4 A[6][2];
#pragma unroll
    for (int u = 0; u < 6; u++) {
      lchar* p = ((u & 1) ? pa1 : pa0) + k * 24576 + (u >> 1) * 128;
      A[u][0] = lds_r128(p); A[u][1] = lds_r128(p + 768);
    }
    if (k == 0) a_dma<NU1, 10>(c, pv);
    if (PFK && w == 3) {
      const u16* pf = ((pb & 1) ? Wk : Wq) + (size_t)(pb >> 1) * 30720 + k * 6144 + l * 8;
#pragma unroll
      for (int i = 0; i < 12; i++) gdma(pf + i * 512, c.lb + 159744);
    }
    f4 acc[2][5];
#pragma unroll
    for (int i = 0; i < 5; i++) { acc[0][i] = (f4){0, 0, 0, 0}; acc[1][i] = (f4){0, 0, 0, 0}; }
#pragma unroll
    for (int u = 0; u < 6; u++)
#pragma unroll
      for (int nt = 0; nt < 5; nt++) {
        acc[0][nt] = mfma32(as_bf8(A[u][0]), as_bf8(B[u][nt]), acc[0][nt]);
        acc[1][nt] = mfma32(as_bf8(A[u][1]), as_bf8(B[u][nt]), acc[1][nt]);
        if (k && nt == 4 && u < 5) {
          __builtin_amdgcn_sched_barrier(0);
          st128(o[u], c.rs, vo, so + u * (2 * DJS) + (k - 1) * 1024);
          __builtin_amdgcn_sched_barrier(0);
        }
      }
    TS(65, 1 + k);
#pragma unroll
    for (int nt = 0; nt < 5; nt++)
      o[nt] = (u4){pack2(acc[0][nt][0], acc[0][nt][1]), pack2(acc[0][nt][2], acc[0][nt][3]), pack2(acc[1][nt][0], acc[1][nt][1]), pack2(acc[1][nt][2], acc[1][nt][3])};
  }
#pragma unroll
  for (int nt = 0; nt < 5; nt++) st128(o[nt], c.rs, vo, so + nt * (2 * DJS) + 4 * 1024);
  TS(65, 6);
  CODE_END("0");
}

DEV void d_load(const Ctx& c, int j, u8v (&D)[4][10]) {
  const unsigned so = OFF_D + j * DJS + c.w * DWVS;
#pragma unroll
  for (int t = 0; t < 4; t++)
#pragma unroll
    for (int kb = 0; kb < 10; kb++) {
      CB();
      u4 a = __builtin_amdgcn_raw_buffer_load_b128(c.rs, c.l * 16, so + (t * 10 + kb) * 2048, DAUX), b = __builtin_amdgcn_raw_buffer_load_b128(c.rs, c.l * 16 + 1024, so + (t * 10 + kb) * 2048, DAUX);
      if (DDEP) wait_vm<DDEP>();
      CB();
      u8v f = (u8v){a[0], a[1], a[2], a[3], b[0], b[1], b[2], b[3]};
      D[t][kb] = f;
    }
}
DEV void role_R(const Ctx& c, int j) {
  lin_dma<10, 0>(c, OFF_XNP, 16384);
  u8v D[4][10];
  d_load(c, j, D);
  const int l = c.l, g = l >> 4, n = l & 15, w = c.w;
  const int h0 = 4 * w + (g >> 1) + 16 * (n >> 3);
  const unsigned hb = (h0 >> 3) * 16384 + (h0 & 7) * 640;
  const unsigned hx = ((w >> 1) + 2 * (n >> 3)) * 16384 + (w & 1) * 2560;
  wait_vm0();
  bar();
  asm volatile(R_ASM :: R_WOPS, "{v176}"(w * 16384 + 5120 + l * 16), "{v177}"(w * 16384 + 7168 + l * 8), "{v178}"(l * 16), "{v179}"(l * 16 + 4096), "{v180}"(l * 16 + 8192),
               "{v181}"(16384 + l * 16), "{v182}"((l & 8) ? 0xEEEE : 0x4444), "{v183}"(hx + g * 128 + (n & 7) * 16),
               "{v184}"(hb + 512 + (g & 1) * 64 + (n & 7) * 8), "{v185}"(j * UXJ + w * 5120 + l * 16),
               "{v187}"(10240 + (w >> 1) * 1280 + (n & 7) * 144 + (g & 1) * 64 + ((w & 1) * 2 + (n >> 3)) * 4), "{v188}"((l >> 3) * 144 + (l & 7) * 16),
               "{v190}"(w * 2560 + l * 16), "{v191}"(w * 2560 + 2048 + l * 8), "{v167}"((j / 20) * 16384 + 5120 + (j % 20) * 128 + (l >> 3) * 16),
               "{s[4:7]}"(c.srdp), "{s[8:11]}"(c.srd), "{s3}"(c.tb), "{s12}"(j), "{s13}"(w), "{s15}"(c.cb), "{s36}"(c.pofs)
               : SCLOB);
}

constexpr int IMG_X = 26624;
DEV void x_dma(const Ctx& c, const u16* Wm, int h, int kb, unsigned buf) {
#pragma unroll
  for (int ii = 0; ii < 7; ii++) {
    const int i = (ii < 6 || c.w < 2) ? ii : 5;
    const int slot = i * 256 + c.tid;
    const int row = slot / 26, cc = slot - row * 26;
    const int k = 64 * kb + swap13(row & 63);
    const u16* src = Wm + (size_t)k * 6144 + h * 192 + (cc < 24 ? cc : 23) * 8;
    if (XROLL || ii < 6 || c.w < 2) gdma(src, c.lb + buf + (i * 256 + c.w * 64) * 16);
  }
}
DEV void pf_block(const Ctx& c, const u16* Wm, int h, int kb) {
#pragma unroll
  for (int i = 0; i < 6; i++) {
    const int slot = i * 256 + c.tid, row = slot / 24, cc = slot - row * 24;
    gdma(Wm + (size_t)(64 * kb + row) * 6144 + h * 192 + cc * 8, c.lb + 159744 + c.w * 1024);
  }
}
DEV void role_X(const Ctx& c, int h, int mat, const u16* Wm) {
  u8v W[3][10];
  const int l = c.l, w = c.w;
  {
    const int lb = qk_lanebase(l, w);
#pragma unroll
    for (int i = 0; i < 6; i++) x_dma(c, Wm, h, i, i * IMG_X);
#pragma unroll
    for (int kb = 0; kb < 10; kb++) {
      if (kb == 0) wait_vm<35>(); else if (kb == 1) wait_vm<28>(); else if (kb < 6) wait_vm<31>(); else if (kb == 6) wait_vm<21>();
      else if (kb == 7) wait_vm<14>(); else if (kb == 8) wait_vm<7>(); else wait_vm0();
      bar();
#pragma unroll
      for (int tt = 0; tt < 3; tt++) W[tt][kb] = toA(qk_frag(c.L + (kb < 6 ? kb : kb - 4) * IMG_X + lb, tt));
      if (kb >= 1 && kb < 6 || kb == 9) bar();
      if (kb == 1) lin_dma<10, 0>(c, OFF_XNP, 0);
      if (kb >= 2 && kb < 6) x_dma(c, Wm, h, kb + 4, kb * IMG_X);
    }
  }
  lin_dma<10, 0>(c, OFF_XNP + 40960, 40960);
  const unsigned fo = (mat ? FL_KX : FL_QX) + h * 4;
  const unsigned ob = (mat ? OFF_KX : OFF_QX) + h * (64 * 12288);
  unsigned buf = 0;
  wait_vm<10>();
  const unsigned t0 = (unsigned)__builtin_amdgcn_s_memrealtime();
  for (int s = 0; s < NSEG; s++) {
    TS(s, 0);
    if (s == 1 || s == 2 || s == NSEG - 1) wait_vm0(); else if (s) wait_vm<13>();
    bar();
    TS(s, 1);
    if (c.tid == 0 && s != 0 && s != 3) st32(1u, c.rs, fo, OFF_FLAG + (s < 3 ? s - 1 : s - 2) * 2048);
    if (XP) pace(t0, (s - XL) * XP);
    if (s + 2 < NSEG) lin_dma<10, 0>(c, OFF_XNP + (s + 2) * 40960, buf ? buf - 40960 : 81920);
    lchar* xb = c.L + buf + l * 16;
    buf = buf == 81920 ? 0 : buf + 40960;
    f4 acc[2][3];
#pragma unroll
    for (int tt = 0; tt < 3; tt++) { acc[0][tt] = (f4){0, 0, 0, 0}; acc[1][tt] = (f4){0, 0, 0, 0}; }
    u4 a00 = lds_r128(xb), a01 = lds_r128(xb + 1024), a10 = lds_r128(xb + 20480), a11 = lds_r128(xb + 21504);
#pragma unroll
    for (int kb = 0; kb < 10; kb++) {
      u4 n00, n01, n10, n11;
      if (kb < 9) { lchar* p = xb + (kb + 1) * 2048; n00 = lds_r128(p); n01 = lds_r128(p + 1024); n10 = lds_r128(p + 20480); n11 = lds_r128(p + 21504); }
#pragma unroll
      for (int t = 0; t < 3; t++) { acc[0][t] = mfma32(lo8(W[t][kb]), as_bf8(a00), acc[0][t]); acc[1][t] = mfma32(lo8(W[t][kb]), as_bf8(a10), acc[1][t]); }
#pragma unroll
      for (int t = 0; t < 3; t++) { acc[0][t] = mfma32(hi8(W[t][kb]), as_bf8(a01), acc[0][t]); acc[1][t] = mfma32(hi8(W[t][kb]), as_bf8(a11), acc[1][t]); }
      if (kb < 9) { a00 = n00; a01 = n01; a10 = n10; a11 = n11; }
    }
    {
      u2 o[2][3];
#pragma unroll
      for (int rt = 0; rt < 2; rt++)
#pragma unroll
        for (int tt = 0; tt < 3; tt++) o[rt][tt] = (u2){pack2(acc[rt][tt][0], acc[rt][tt][1]), pack2(acc[rt][tt][2], acc[rt][tt][3])};
      const bool od = w & 1;
#pragma unroll
      for (int rt = 0; rt < 2; rt++) {
        const u2 a = od ? o[rt][1] : o[rt][0], b = od ? o[rt][2] : o[rt][1];
        st128((u4){a[0], a[1], b[0], b[1]}, c.rs, l * 16, ob + s * 12288 + rt * 6144 + ((3 * w + 1) >> 1) * 1024);
      }
      const u2 a = od ? o[0][0] : o[0][2], b = od ? o[1][0] : o[1][2];
      st128((u4){a[0], a[1], b[0], b[1]}, c.rs, l * 16, ob + s * 12288 + (w & 1) * 6144 + (1 + 3 * (w >> 1)) * 1024);
    }
  }
  wait_vm0();
  bar();
  if (c.tid < 2) st32(1u, c.rs, fo + c.tid * 2048, OFF_FLAG + (NSEG - 2) * 2048);
}

DEV void role_O(const Ctx& c, int o, int s0, int s1, const u16* x, u16* out) {
  bool dead = false;
  lchar* RED = c.L;
  const int l = c.l, g = l >> 4, n = l & 15, w = c.w;
  const unsigned zxo = (4 * o + (n >> 3)) * UXJ + w * 5120 + ((n & 7) + 16 * g) * 16;
  const unsigned zmo = zxo + 4096;
  const unsigned pxo = 4 * w * 2560 + l * 16;
  const unsigned pmo = (4 * w + (g >> 1)) * 2560 + 1024 + (g & 1) * 128 + n * 8;
  unsigned tprev = 0, tint = 0;
  if (s0 >= NSEG - 2 && w == 0) {
    for (int i = 0; i < SPIN_MAX; ++i) {
      unsigned f = 1;
      if (l == 0) f = ld32(c.rs, o * 128, OFF_PXF + (NSEG - 3) * 4096);
      if (__all(f != 0)) break;
      __builtin_amdgcn_s_sleep(32);
    }
    tprev = (unsigned)__builtin_amdgcn_s_memrealtime(); tint = TDEL;
  }
  for (int s = s0; s < s1; s += 2) {
    TS(s, 0);
    if (w == 0 && !dead) {
      const unsigned fz = l < 32 ? OFF_PXF + l * 128 + (s < NSEG - 2 ? s + 1 : s) * 4096
                        : (l < 36 && s >= NSEG - 2) ? OFF_ZF + (4 * o + l - 32) * 128 : OFF_FLAG + FL_B + (o >> 1) * 4 + s * 2048;
      for (int i = 0; i < 256 && (unsigned)__builtin_amdgcn_s_memrealtime() - tprev < tint; i++) __builtin_amdgcn_s_sleep(2);
      int i = 0;
      for (; i < SPIN_MAX; ++i) {
        unsigned f = 1;
        if (l < 37) f = ld32(c.rs, fz, 0);
        if (__all(f != 0)) break;
        __builtin_amdgcn_s_sleep(OSL);
      }
      dead = i == SPIN_MAX;
      const unsigned tn = (unsigned)__builtin_amdgcn_s_memrealtime();
      tint = (s > s0 && i > 0) ? max(min(tn - tprev, 1000u), (unsigned)OEARLY) - OEARLY : 0;
      tprev = tn;
    }
    bar();
    TS(s, 1);
    u4 ZX[2][4][2], ZM[2][2];
#pragma unroll
    for (int ct = 0; ct < 2; ct++) {
#pragma unroll
      for (int t = 0; t < 4; t++)
#pragma unroll
        for (int hs = 0; hs < 2; hs++) ZX[ct][t][hs] = ld128(c.rs, zxo + t * 1024 + hs * 128, OFF_UX + (s + 1) * 20480 + ct * (2 * UXJ));
#pragma unroll
      for (int hs = 0; hs < 2; hs++) ZM[ct][hs] = ld128(c.rs, zmo + hs * 128, OFF_UX + (s + 1) * 20480 + ct * (2 * UXJ));
    }
    const int row = 32 * s + 16 * (n >> 3) + 8 * (w & 1) + (n & 7);
    const unsigned eo = row * 640 + o * 32 + (w >> 1) * 16 + g * 4;
    const u2 xv = *(const u2*)(x + eo), bv = ld64(c.rs, eo * 2, OFF_B);
    const unsigned so = OFF_PX + s * (32 * 2560);
    u4 P[4][2][2]; u2 PM[2][2][2];
#pragma unroll
    for (int t = 0; t < 4; t++)
#pragma unroll
      for (int hs = 0; hs < 2; hs++)
#pragma unroll
        for (int rt = 0; rt < 2; rt++) P[t][hs][rt] = ld128(c.rs, pxo + (t + 16 * hs) * 2560 + rt * 1280, so);
#pragma unroll
    for (int pp = 0; pp < 2; pp++)
#pragma unroll
      for (int hs = 0; hs < 2; hs++)
#pragma unroll
        for (int rt = 0; rt < 2; rt++) PM[pp][hs][rt] = ld64(c.rs, pmo + (2 * pp + 16 * hs) * 2560 + rt * 1280, so);
    __builtin_amdgcn_sched_barrier(0);
    f4 acc[2][2];
#pragma unroll
    for (int i = 0; i < 2; i++) { acc[i][0] = (f4){0, 0, 0, 0}; acc[i][1] = (f4){0, 0, 0, 0}; }
#pragma unroll
    for (int t = 0; t < 4; t++)
#pragma unroll
      for (int hs = 0; hs < 2; hs++)
#pragma unroll
        for (int ct = 0; ct < 2; ct++)
#pragma unroll
          for (int rt = 0; rt < 2; rt++) acc[ct][rt] = mfma32(as_bf8(ZX[ct][t][hs]), as_bf8(P[t][hs][rt]), acc[ct][rt]);
#pragma unroll
    for (int hs = 0; hs < 2; hs++)
#pragma unroll
      for (int ct = 0; ct < 2; ct++)
#pragma unroll
        for (int rt = 0; rt < 2; rt++)
          acc[ct][rt] = mfma32(as_bf8(ZM[ct][hs]), as_bf8((u4){PM[0][hs][rt][0], PM[0][hs][rt][1], PM[1][hs][rt][0], PM[1][hs][rt][1]}), acc[ct][rt]);
    lchar* rb = RED + (s & 2) * 8192 + l * 16;
#pragma unroll
    for (int ct = 0; ct < 2; ct++)
#pragma unroll
      for (int rt = 0; rt < 2; rt++) lds_w128(rb + ((ct * 2 + rt) * 4 + w) * 1024, __builtin_bit_cast(u4, acc[ct][rt]));
    bar();
    {
      f4 t = {0, 0, 0, 0};
#pragma unroll
      for (int i = 0; i < 4; i++) t += __builtin_bit_cast(f4, lds_r128(rb + (w * 4 + i) * 1024));
      float r0 = bfr(bflo(xv[0]) + bfr(t[0])) + bflo(bv[0]);
      float r1 = bfr(bfhi(xv[0]) + bfr(t[1])) + bfhi(bv[0]);
      float r2 = bfr(bflo(xv[1]) + bfr(t[2])) + bflo(bv[1]);
      float r3 = bfr(bfhi(xv[1]) + bfr(t[3])) + bfhi(bv[1]);
      u2 ov = {pack2(r0, r1), pack2(r2, r3)};
      *(u2*)(out + eo) = ov;
    }
    TS(s, 3);
  }
}

constexpr int IMG_G1 = 36864;
DEV void g1_dma(const Ctx& c, const u16* Wg, const u16* Wu, int cu, int kb, unsigned buf) {
#pragma unroll
  for (int i = 0; i < 9; i++) {
    const int slot = i * 256 + c.tid;
    const int row = slot / 18, cc = slot - row * 18;
    const int k = 64 * kb + swap13(row & 63);
    const u16* src = ((row >> 6) ? Wu : Wg) + (size_t)k * 1536 + cu * 128 + (cc < 16 ? cc : 15) * 8;
    gdma(src, c.lb + buf + (i * 256 + c.w * 64) * 16);
  }
}
DEV void role_G1(const Ctx& c, int cu, int par, const u16* Wg, const u16* Wu) {
  u8v W[4][10];
  const int l = c.l, g = l >> 4, w = c.w;
  {
    const int q = (l >> 2) & 3, p = l & 3;
    const int lb = (16 * (g >> 1) + 4 * (g & 1) + q) * 288 + (32 * w + 4 * p) * 2;
#pragma unroll
    for (int i = 0; i < 4; i++) g1_dma(c, Wg, Wu, cu, i, i * IMG_G1);
#pragma unroll
    for (int kb = 0; kb < 10; kb++) {
      if (kb + 3 < 10) wait_vm<27>(); else wait_vm0();
      bar();
      lchar* bp = c.L + (kb & 3) * IMG_G1 + lb;
#pragma unroll
      for (int tt = 0; tt < 4; tt++) {
        const int o = (tt >> 1) * 64 * 288 + (tt & 1) * 32;
        u2 a = trread(bp + o), b = trread(bp + o + 8 * 288), cc = trread(bp + o + 32 * 288), d = trread(bp + o + 40 * 288);
        u8v f = (u8v){a[0], a[1], b[0], b[1], cc[0], cc[1], d[0], d[1]};
        W[tt][kb] = (kb < 7) ? toA(f) : f;
      }
      bar();
      if (kb + 4 < 10) g1_dma(c, Wg, Wu, cu, kb + 4, (kb & 3) * IMG_G1);
    }
  }
  lin_dma<10, 0>(c, OFF_XNP + par * 40960, 0);
  wait_vm0();
  const unsigned t0 = (unsigned)__builtin_amdgcn_s_memrealtime();
  for (int s = par; s < NSEG; s += 2) {
    TS(s, 0);
    wait_vm<2>();
    bar();
    TS(s, 1);
    if (s > 3 && c.tid == 0) st32(1u, c.rs, FL_H + cu * 4, OFF_FLAG + (s - 4) * 2048);
    if (GP) pace(t0, (s - GL) * GP);
    if (s + 2 < NSEG) lin_dma<10, 0>(c, OFF_XNP + (s + 2) * 40960, ((s + 2) & 2) * 20480);
    lchar* xb = c.L + (s & 2) * 20480 + l * 16;
    f4 acc[2][4];
#pragma unroll
    for (int t = 0; t < 4; t++) { acc[0][t] = (f4){0, 0, 0, 0}; acc[1][t] = (f4){0, 0, 0, 0}; }
    u4 a00 = lds_r128(xb), a01 = lds_r128(xb + 1024), a10 = lds_r128(xb + 20480), a11 = lds_r128(xb + 21504);
#pragma unroll
    for (int kb = 0; kb < 10; kb++) {
      u4 n00, n01, n10, n11;
      if (kb < 9) { lchar* p = xb + (kb + 1) * 2048; n00 = lds_r128(p); n01 = lds_r128(p + 1024); n10 = lds_r128(p + 20480); n11 = lds_r128(p + 21504); }
#pragma unroll
      for (int t = 0; t < 4; t++) { acc[0][t] = mfma32(lo8(W[t][kb]), as_bf8(a00), acc[0][t]); acc[1][t] = mfma32(lo8(W[t][kb]), as_bf8(a10), acc[1][t]); }
#pragma unroll
      for (int t = 0; t < 4; t++) { acc[0][t] = mfma32(hi8(W[t][kb]), as_bf8(a01), acc[0][t]); acc[1][t] = mfma32(hi8(W[t][kb]), as_bf8(a11), acc[1][t]); }
      if (kb < 9) { a00 = n00; a01 = n01; a10 = n10; a11 = n11; }
    }
#pragma unroll
    for (int rt = 0; rt < 2; rt++) {
      float hv[8];
#pragma unroll
      for (int t = 0; t < 2; t++)
#pragma unroll
        for (int i = 0; i < 4; i++) {
          float gg = bfr(acc[rt][t][i]), uu = bfr(acc[rt][2 + t][i]);
          gg = gg * (gg > 0.f ? 1.f : 0.f);
          hv[4 * t + i] = gg * uu;
        }
      u4 o = {pack2(hv[0], hv[1]), pack2(hv[2], hv[3]), pack2(hv[4], hv[5]), pack2(hv[6], hv[7])};
      st128(o, c.rs, (4 * cu + w) * 1024 + l * 16, OFF_H + (s * 2 + rt) * 49152);
    }
  }
  wait_vm0();
  bar();
  if (c.tid < 2) st32(1u, c.rs, FL_H + cu * 4 + c.tid * 4096, OFF_FLAG + (NSEG - 4 + par) * 2048);
}

constexpr int IMG_G2 = 40960;
DEV void g2_dma(const Ctx& c, const u16* Wd, int cu, int ch, unsigned buf) {
#pragma unroll
  for (int i = 0; i < 10; i++) {
    const int slot = i * 256 + c.tid;
    const int row = slot / 10, cc = slot - row * 10;
    const u16* src = Wd + (size_t)(256 * ch + row) * 640 + cu * 64 + (cc < 8 ? cc : 7) * 8;
    gdma(src, c.lb + buf + (i * 256 + c.w * 64) * 16);
  }
}
DEV void g2_wait(const Ctx& c, int s, int& ready, bool& dead) {
  if (s < ready || dead) return;
  const int l = c.l < 60 ? c.l : 59;
  const int ds = l / 12, cu = l - ds * 12;
  const unsigned vo = (s + ds < NSEG ? ds : 0) * 2048 + FL_H + cu * 4;
  for (int i = 0; i < SPIN_MAX; ++i) {
    unsigned f = ld32(c.rs, vo, OFF_FLAG + s * 2048);
    const unsigned long long z = __ballot(f == 0);
    const int lead = z ? __builtin_ctzll(z) / 12 : 5;
    if (lead) { ready = s + lead; return; }
    __builtin_amdgcn_s_sleep(8);
  }
  dead = true;
}
DEV u4 g2_ld1(const Ctx& c, int s, int i) {
  return __builtin_amdgcn_raw_buffer_load_b128(c.rs, c.w * 2048 + c.l * 16 + (i & 1) * 1024, OFF_H + s * 98304 + (i >> 1) * 8192, 16);
}
DEV void g2_ld(const Ctx& c, int s, u4 (&H)[24]) {
#pragma unroll
  for (int i = 0; i < 24; i++) H[i] = g2_ld1(c, s, i);
  __builtin_amdgcn_sched_barrier(0);
}
DEV void g2_seg(const Ctx& c, int cu, int s, int sn, const u4 (&W)[4][12], u4 (&H)[24]) {
  const int l = c.l, g = l >> 4, n = l & 15, w = c.w;
  f4 acc[2][4];
#pragma unroll
  for (int i = 0; i < 4; i++) { acc[0][i] = (f4){0, 0, 0, 0}; acc[1][i] = (f4){0, 0, 0, 0}; }
#pragma unroll
  for (int rt = 0; rt < 2; rt++)
#pragma unroll
    for (int u = 0; u < 12; u++) {
#pragma unroll
      for (int ct = 0; ct < 4; ct++) acc[rt][ct] = mfma32(as_bf8(W[ct][u]), as_bf8(H[rt * 12 + u]), acc[rt][ct]);
      __builtin_amdgcn_sched_barrier(0);
      H[rt * 12 + u] = g2_ld1(c, sn, rt * 12 + u);
      __builtin_amdgcn_sched_barrier(0);
    }
  lchar* rb = c.L + (s & 1) * 32768 + l * 16;
#pragma unroll
  for (int rt = 0; rt < 2; rt++)
#pragma unroll
    for (int ct = 0; ct < 4; ct++) lds_w128(rb + ((ct * 2 + rt) * 4 + w) * 1024, __builtin_bit_cast(u4, acc[rt][ct]));
  wait_vm<24>();
  bar();
  if (s > 0 && c.tid == 0) st32(1u, c.rs, FL_B + cu * 4, OFF_FLAG + (s - 1) * 2048);
  {
    const int nn = 8 * (w >> 1) + (l >> 3), cc = l & 7;
    lchar* q = c.L + (s & 1) * 32768 + (((cc >> 1) * 2 + (w & 1)) * 4) * 1024 + (32 * (cc & 1) + nn) * 16;
    f4 t0 = {0, 0, 0, 0}, t1 = {0, 0, 0, 0};
#pragma unroll
    for (int i = 0; i < 4; i++) { t0 += __builtin_bit_cast(f4, lds_r128(q + i * 1024)); t1 += __builtin_bit_cast(f4, lds_r128(q + i * 1024 + 256)); }
    const int row = 32 * s + 16 * (w >> 1) + 8 * (w & 1) + (l >> 3);
    u4 o = {pack2(t0[0], t0[1]), pack2(t0[2], t0[3]), pack2(t1[0], t1[1]), pack2(t1[2], t1[3])};
    st128(o, c.rs, (row * 640 + cu * 64 + cc * 8) * 2, OFF_B);
  }
}
DEV void role_G2(const Ctx& c, int cu, const u16* Wd) {
  bool dead = false;
  u4 W[4][12];
  const int l = c.l, g = l >> 4, w = c.w;
  {
    const int q = (l >> 2) & 3, p = l & 3;
    const int lb = (64 * w + 4 * g + q) * 160 + 8 * p;
#pragma unroll
    for (int i = 0; i < 3; i++) g2_dma(c, Wd, cu, i, i * IMG_G2);
#pragma unroll
    for (int ch = 0; ch < 6; ch++) {
      if (ch + 2 < 6) wait_vm<20>(); else wait_vm0();
      bar();
      lchar* bp = c.L + (ch % 3) * IMG_G2 + lb;
#pragma unroll
      for (int e = 0; e < 2; e++)
#pragma unroll
        for (int ct = 0; ct < 4; ct++) {
          u2 a = trread(bp + e * 32 * 160 + ct * 32), b = trread(bp + (e * 32 + 16) * 160 + ct * 32);
          W[ct][ch * 2 + e] = toA4((u4){a[0], a[1], b[0], b[1]});
        }
      bar();
      if (ch + 3 < 6) g2_dma(c, Wd, cu, ch + 3, (ch % 3) * IMG_G2);
    }
  }
  int ready = 0;
  u4 HA[24], HB[24];
  g2_wait(c, 0, ready, dead);
  g2_ld(c, 0, HA);
  g2_wait(c, 1, ready, dead);
  g2_ld(c, 1, HB);
  for (int s = 0; s < NSEG; s += 2) {
    const int sn = s + 2 < NSEG ? s + 2 : s;
    TS(s, 0);
    g2_wait(c, sn, ready, dead);
    CB();
    TS(s, 1);
    g2_seg(c, cu, s, sn, W, HA);
    TS(s + 1, 0);
    g2_wait(c, sn + 1, ready, dead);
    CB();
    TS(s + 1, 1);
    g2_seg(c, cu, s + 1, sn + 1, W, HB);
  }
  wait_vm0();
  __builtin_amdgcn_s_waitcnt(0);
  bar();
  if (c.tid == 0) st32(1u, c.rs, FL_B + cu * 4, OFF_FLAG + (NSEG - 1) * 2048);
}

DEV void role_V(const Ctx& c, int v, unsigned t0) {
  unsigned xcc; asm volatile("s_getreg_b32 %0, hwreg(HW_REG_XCC_ID)" : "=s"(xcc));
  const char* pp = c.ws + c.cb + (v * 4 + c.w) * 81920 + 7808 + (c.l & 7) * 16;
  u4 a0;
#pragma unroll
  for (int r = 0; r < 2; r++)
    asm volatile("global_load_dwordx4 %0, %1, off sc1\n\ts_waitcnt vmcnt(0)" : "=&v"(a0) : "v"(pp + 256 + r * (2 * MB)) : "memory");
  for (int i = 0; i < 4096 && (unsigned)__builtin_amdgcn_s_memrealtime() - t0 < VDEL; i++) __builtin_amdgcn_s_sleep(8);
  unsigned t[4] = {~0u, ~0u, ~0u, ~0u};
#pragma unroll
  for (int r = 0; r < 4 * NVOTE; r++) {
    const char* q = pp + (r & 1) * 8192 + ((r >> 1) & 1) * (2 * MB) + ((r >> 2) & 1) * 128 + (r >> 3) * 16384;
    unsigned long long c0, c1;
    asm volatile("s_memtime %0\n\ts_waitcnt lgkmcnt(0)\n\tglobal_load_dwordx4 %2, %3, off sc1\n\ts_waitcnt vmcnt(0)\n\ts_memtime %1\n\ts_waitcnt lgkmcnt(0)"
                 : "=&s"(c0), "=&s"(c1), "=&v"(a0) : "v"(q) : "memory");
    t[r & 3] = min(t[r & 3], (unsigned)(c1 - c0));
  }
  if (c.l == 0) {
    const unsigned inv = (xcc >> 2) & 1;
    const unsigned add = 0x10000u | ((t[0] > t[1] ? 1u : 0u) ^ inv) | (((t[2] > t[3] ? 1u : 0u) ^ inv) << 8);
    const unsigned tot = add + __hip_atomic_fetch_add((unsigned*)(c.ws + OFF_TICK + 128), add, __ATOMIC_RELAXED, __HIP_MEMORY_SCOPE_AGENT);
    if ((tot >> 16) == 24)
      st32(0x80000000u | ((tot & 255) > 12 ? 1u : 0u) | (((tot >> 8) & 255) > 12 ? 2u : 0u), c.rs, 0, c.cb + (SWSTEP - 2) * 65536 + 7680 + 64);
  }
}

__global__ void __launch_bounds__(256, 1) k_main(const u16* __restrict__ x, const u16* __restrict__ Wq, const u16* __restrict__ Wk,
                                                 const u16* __restrict__ Wg, const u16* __restrict__ Wu, const u16* __restrict__ Wd,
                                                 u16* __restrict__ out, char* __restrict__ ws) {
  __shared__ __attribute__((aligned(16))) char smem[163840];
  Ctx c;
  c.L = (lchar*)smem;
  c.lb = (unsigned)(uintptr_t)c.L;
  c.ws = ws;
  c.rs = __builtin_amdgcn_make_buffer_rsrc(ws, 0, WS_USED, 0x00020000);
  c.srd = (u4){(unsigned)(uintptr_t)ws, (unsigned)((uintptr_t)ws >> 32) & 0xFFFFu, WS_USED, 0x00020000u};
  c.w = __builtin_amdgcn_readfirstlane(threadIdx.x >> 6);
  c.srdp = c.srd; c.srdp[2] = 256u * MB;
#define LN do { int l_; asm volatile("v_mbcnt_lo_u32_b32 %0, -1, 0\n\tv_mbcnt_hi_u32_b32 %0, -1, %0" : "=v"(l_)); c.l = l_; c.tid = c.w * 64 + l_; } while (0)
  LN;
  if (CPF & 2) CODE_PF("1", ((blockIdx.x >> 3) & 3) * 4 + c.w, 16, 159744 + c.w * 1024);
  int slot;
  {
    unsigned xcc; asm volatile("s_getreg_b32 %0, hwreg(HW_REG_XCC_ID)" : "=s"(xcc));
    xcc &= 7;
    c.tb = __builtin_amdgcn_readfirstlane(*(const unsigned*)(ws + OFF_VOTE + 1024));
    if (c.w == 0) {
      int t = 0;
      if (c.l == 0) t = __hip_atomic_fetch_add((int*)(ws + OFF_TICK + xcc * 256), 1, __ATOMIC_RELAXED, __HIP_MEMORY_SCOPE_AGENT);
      t = __builtin_amdgcn_readfirstlane(t);
      int sl = xcc * 32 + t;
      if (t >= 32) {
        sl = 125;
        for (int i = 0; i < SPIN_MAX; i++) {
          int cnt[8], tot = 0;
#pragma unroll
          for (int k = 0; k < 8; k++) { cnt[k] = __builtin_amdgcn_readfirstlane(ld32(c.rs, 0, OFF_TICK + k * 256)); tot += cnt[k]; }
          if (tot == 256) {
            int rank = t - 32;
#pragma unroll
            for (int k = 0; k < 8; k++) if (k < (int)xcc && cnt[k] > 32) rank += cnt[k] - 32;
#pragma unroll
            for (int k = 0; k < 8; k++) {
              const int fr = cnt[k] < 32 ? 32 - cnt[k] : 0;
              if (rank >= 0 && rank < fr) sl = k * 32 + cnt[k] + rank;
              rank -= fr;
            }
            break;
          }
          __builtin_amdgcn_s_sleep(8);
        }
      }
      if (c.l == 0) *(__attribute__((address_space(3))) int*)c.L = sl;
    }
    bar();
    slot = __builtin_amdgcn_readfirstlane(*(__attribute__((address_space(3))) int*)c.L);
    c.cb = comm_base(ws);
    bar();
  }
  const int xg = slot >> 5, t = slot & 31;
  c.id = slot < 128 ? (t < 8 ? xg * 8 + t : t < 28 ? 32 + xg * 20 + t - 8 : t < 31 ? 240 + xg * 3 + t - 28 : 252 + xg)
                    : (t < 16 ? 48 + xg * 16 + t : t < 26 ? 120 + xg * 10 + t : 166 + xg * 6 + t);
  if (GSW) {
    if (c.id >= 240 && c.id < 250) c.id = 216 + (c.id - 240) * 12 / 5;
    else if (c.id >= 216 && c.id < 240) { const int k = (c.id - 216) * 5 + 11; if ((k / 12) * 12 / 5 == c.id - 216) c.id = 240 + k / 12; }
  }
  c.pofs = 224u * MB + (c.id * 4 + c.w) * (68 * 64);
  TS(67, 0);
  TC(67, 2);
  const int b = c.id;
  const unsigned tp = (unsigned)__builtin_amdgcn_s_memrealtime();
  {
    if (HS && b >= 240) {
      LN;
      for (int n = 0; n < 4 * HS; n++) {
        const int ks = 9 - (n >> 2);
        pf_block(c, (n & 1) ? Wk : Wq, xg * 8 + 2 * (t - 28) + ((n >> 1) & 1), ks < 8 ? ks + (ks + 2) / 4 : 5 * ks - 38);
      }
    }
    if (HX && b >= 176 && b < 216) {
      LN;
      for (int n = t - 16; n < 16 * HX; n += 10) pf_block(c, (n & 8) ? Wq : Wk, (xg - 4) * 8 + (n & 7), 10 - HX + (n >> 4));
    }
    if (RDEL || GDEL || QDEL) pace(tp, b < 32 ? 0 : b < 112 ? RDEL : b < 176 ? ((b & 1) ? 0 : QDEL) : b < 240 ? GDEL : b < 250 ? G2DEL : 0);
  }
  if (b < 32) { LN; role_S(c, b, Wq, Wk); }
  else if (b < 112) { LN; role_R(c, b - 32); }
  else if (b < 176) { LN; role_X(c, (b - 112) >> 1, b & 1, (b & 1) ? Wk : Wq); }
  else if (b < 216) {}
  else if (b < 240) { LN; role_G1(c, (b - 216) >> 1, b & 1, Wg, Wu); }
  else if (b < 250) { LN; role_G2(c, b - 240, Wd); }
  if (b >= 136 && b < 216) {
    LN;
    const bool tl = b < 176;
    role_O(c, (b - (tl ? 136 : 176)) >> 1, (tl ? NSEG - 2 : 0) + (b & 1), tl ? NSEG : NSEG - 2, x, out);
  }
  if (b >= 250) { LN; role_V(c, b - 250, tp); }
  TS(67, 1);
  TC(67, 3);
  CODE_END("1");
}

extern "C" void kernel_launch(void* const* d_in, const int* in_sizes, int n_in, void* d_out, int out_size, void* d_ws, size_t ws_size,
                              hipStream_t stream) {
  const u16* x = (const u16*)d_in[0];
  const u16* mem0 = (const u16*)d_in[1];
  const u16* Wq = (const u16*)d_in[2];
  const u16* Wk = (const u16*)d_in[3];
  const u16* Wv = (const u16*)d_in[4];
  const u16* Wo = (const u16*)d_in[5];
  const u16* Wg = (const u16*)d_in[8];
  const u16* Wu = (const u16*)d_in[9];
  const u16* Wd = (const u16*)d_in[10];
  char* ws = (char*)d_ws;
  k_prep<<<256, 256, 0, stream>>>(x, mem0, Wv, Wo, Wq, Wk, ws);
  k_main<<<256, 256, 0, stream>>>(x, Wq, Wk, Wg, Wu, Wd, (u16*)d_out, ws);
}
```
